```python
import torch

def _softmax(x, axis=-1):
    return torch.softmax(x, dim=axis)
def _gelu(x):
    return torch.nn.functional.gelu(x, approximate="tanh")
def _silu(x):
    return torch.nn.functional.silu(x)
def _sigmoid(x):
    return torch.sigmoid(x)
def _layernorm(x, axis=-1, eps=1e-5):
    m = x.mean(dim=axis, keepdim=True)
    v = x.var(dim=axis, keepdim=True, unbiased=False)
    return (x - m) / torch.sqrt(v + eps)
def _scatter_add(tgt, idx, src, axis=0):
    return tgt.clone().index_add_(axis, idx, src)
def _segment_sum(src, seg, n):
    out = torch.zeros((n,) + src.shape[1:], dtype=src.dtype)
    return out.index_add_(0, seg, src)
def _pad(x, pw):
    flat = tuple(v for pair in reversed(pw) for v in pair)
    return torch.nn.functional.pad(x, flat)

kernel_name = 'concat_heads__linear_attn_block__linear_attn_block__prompt'

def setup_inputs():

    _w = (lambda a: (lambda t: t.to(torch.bfloat16) if t.is_floating_point() else t)(torch.as_tensor(a)))
    def _A_setup():
        B, S, D = 1, 2048, 512
        return {
            "x": _w((lambda *s: torch.randn(*s, dtype=torch.bfloat16) * 0.05)(B, S, D)),
            "wqkv": _w((lambda *s: torch.randn(*s, dtype=torch.bfloat16) * 0.4)(D, 3 * D)),
            "wo": _w((lambda *s: torch.randn(*s, dtype=torch.bfloat16) * 0.05)(D, D)),
        }
    _A_in = _A_setup()

    def _B_setup():
        B, S, D = 1, 2048, 512
        return {
            "x": _w((lambda *s: torch.randn(*s, dtype=torch.bfloat16) * 0.05)(B, S, D)),
            "wqkv": _w((lambda *s: torch.randn(*s, dtype=torch.bfloat16) * 0.4)(D, 3 * D)),
            "wo": _w((lambda *s: torch.randn(*s, dtype=torch.bfloat16) * 0.05)(D, D)),
        }
    _B_in = _B_setup()

    return {
        "A_x": _A_in["x"],
        "A_wqkv": _A_in["wqkv"],
        "A_wo": _A_in["wo"],
        "B_x": _B_in["x"],
        "B_wqkv": _B_in["wqkv"],
        "B_wo": _B_in["wo"],
    }

def reference(A_x, A_wqkv, A_wo, B_x, B_wqkv, B_wo):
    def _A_ref(x, wqkv, wo):
        B, S, D = x.shape
        H = 32; Dh = D // H
        qkv = (lambda x, *d: x.permute(*d))((x @ wqkv).reshape(B, S, 3, H, Dh), 2, 0, 3, 1, 4)
        q, k, v = qkv[0], qkv[1], qkv[2]
        phi = lambda t: torch.where(t > 0, t + 1, torch.exp(t)) + 1e-6
        pq, pk = phi(q), phi(k)
        kv = torch.einsum("bhsd,bhse->bhde", pk, v)
        z = torch.einsum("bhsd->bhd", pk)
        num = torch.einsum("bhqd,bhde->bhqe", pq, kv)
        den = torch.einsum("bhqd,bhd->bhq", pq, z)[..., None] + 1e-6
        o = num / den
        return (lambda x, *d: x.permute(*d))(o, 0, 2, 1, 3).reshape(B, S, D) @ wo

    def _B_ref(x, wqkv, wo):
        B, S, D = x.shape
        H = 32; Dh = D // H
        qkv = (lambda x, *d: x.permute(*d))((x @ wqkv).reshape(B, S, 3, H, Dh), 2, 0, 3, 1, 4)
        q, k, v = qkv[0], qkv[1], qkv[2]
        phi = lambda t: torch.where(t > 0, t + 1, torch.exp(t)) + 1e-6
        pq, pk = phi(q), phi(k)
        kv = torch.einsum("bhsd,bhse->bhde", pk, v)
        z = torch.einsum("bhsd->bhd", pk)
        num = torch.einsum("bhqd,bhde->bhqe", pq, kv)
        den = torch.einsum("bhqd,bhd->bhq", pq, z)[..., None] + 1e-6
        o = num / den
        return (lambda x, *d: x.permute(*d))(o, 0, 2, 1, 3).reshape(B, S, D) @ wo

    a = _A_ref(A_x, A_wqkv, A_wo)
    b = _B_ref(B_x, B_wqkv, B_wo)
    return torch.concatenate([a, b], axis=-1)
```

```cpp
#include <hip/hip_runtime.h>
#include <hip/hip_bf16.h>
typedef unsigned short u16;
__device__ __forceinline__ float bf2f(u16 v){ return __uint_as_float(((unsigned)v)<<16); }
__device__ __forceinline__ u16 f2bf(float f){ unsigned u=__float_as_uint(f); u += 0x7fffu + ((u>>16)&1u); return (u16)(u>>16); }
__device__ __forceinline__ float rbf(float f){ return bf2f(f2bf(f)); }
__device__ __forceinline__ float phi(float t){
  float r = t>0.f ? rbf(t+1.f) : rbf(__expf(t));
  return rbf(r + 1e-6f);
}
constexpr int S=2048, D=512, H=32, DH=16;
__global__ void k_qkv(const u16* x0,const u16* w0,const u16* x1,const u16* w1,u16* qkv){
  int p=blockIdx.z; const u16* x=p?x1:x0; const u16* w=p?w1:w0;
  int n=blockIdx.x*256+threadIdx.x; int s=blockIdx.y;
  float a=0; for(int k=0;k<D;k++) a+=bf2f(x[s*D+k])*bf2f(w[k*3*D+n]);
  float t=rbf(a);
  if(n<2*D) t=phi(t);
  qkv[((size_t)p*S+s)*3*D+n]=f2bf(t);
}
__global__ void k_kv(const u16* qkv,float* kv){
  int h=blockIdx.x,p=blockIdx.y,t=threadIdx.x;
  const u16* b=qkv+(size_t)p*S*3*D;
  float a=0;
  if(t<256){int d=t>>4,e=t&15; for(int s=0;s<S;s++) a+=bf2f(b[s*3*D+D+h*16+d])*bf2f(b[s*3*D+2*D+h*16+e]);}
  else {int d=t-256; for(int s=0;s<S;s++) a+=bf2f(b[s*3*D+D+h*16+d]);}
  kv[(p*H+h)*272+t]=rbf(a);
}
__global__ void k_o(const u16* qkv,const float* kv,u16* o){
  int s=blockIdx.x,p=blockIdx.y,c=threadIdx.x,h=c>>4,e=c&15;
  const u16* q=qkv+((size_t)p*S+s)*3*D+h*16; const float* k=kv+(p*H+h)*272;
  float num=0,den=0; for(int d=0;d<16;d++){float pq=bf2f(q[d]); num+=pq*k[d*16+e]; den+=pq*k[256+d];}
  num=rbf(num); den=rbf(rbf(den)+1e-6f);
  o[((size_t)p*S+s)*D+c]=f2bf(num/den);
}
__global__ void k_out(const u16* o,const u16* wo0,const u16* wo1,u16* out){
  int s=blockIdx.x,p=blockIdx.y,n=threadIdx.x; const u16* w=p?wo1:wo0;
  const u16* a=o+((size_t)p*S+s)*D; float acc=0; for(int k=0;k<D;k++) acc+=bf2f(a[k])*bf2f(w[k*D+n]);
  out[(size_t)s*2*D+p*D+n]=f2bf(acc);
}
extern "C" void kernel_launch(void* const* d_in, const int* in_sizes, int n_in, void* d_out, int out_size, void* d_ws, size_t ws_size, hipStream_t stream){
  const u16* x0=(const u16*)d_in[0]; const u16* w0=(const u16*)d_in[1]; const u16* wo0=(const u16*)d_in[2];
  const u16* x1=(const u16*)d_in[3]; const u16* w1=(const u16*)d_in[4]; const u16* wo1=(const u16*)d_in[5];
  u16* qkv=(u16*)d_ws; float* kv=(float*)((char*)d_ws+(size_t)2*S*3*D*2); u16* o=(u16*)((char*)kv+2*H*272*4);
  k_qkv<<<dim3(6,S,2),256,0,stream>>>(x0,w0,x1,w1,qkv);
  k_kv<<<dim3(H,2),272,0,stream>>>(qkv,kv);
  k_o<<<dim3(S,2),512,0,stream>>>(qkv,kv,o);
  k_out<<<dim3(S,2),512,0,stream>>>(o,wo0,wo1,(u16*)d_out);
}
```

```cpp
#include <hip/hip_runtime.h>
typedef unsigned short u16;
typedef __attribute__((ext_vector_type(8))) __bf16 bf16x8;
typedef __attribute__((ext_vector_type(2))) __bf16 bf16x2;
typedef __attribute__((ext_vector_type(2))) float f32x2;
typedef __attribute__((ext_vector_type(4))) float f32x4;
typedef __attribute__((ext_vector_type(16))) float f32x16;
typedef __attribute__((ext_vector_type(4))) short s16x4;
typedef __attribute__((ext_vector_type(4))) unsigned u32x4;
typedef __attribute__((ext_vector_type(2))) unsigned u32x2;
typedef __attribute__((address_space(3))) void* ldsp;
typedef __attribute__((address_space(3))) const char* ldscp;
#define DI __device__ __forceinline__

template<int N> DI void waitv(){ asm volatile("s_waitcnt vmcnt(%0)"::"n"(N):"memory"); }
DI void waitl0(){ asm volatile("s_waitcnt lgkmcnt(0)":::"memory"); }
DI void bar(){ __builtin_amdgcn_s_barrier(); }
DI unsigned pk2(float a,float b){ f32x2 v={a,b}; bf16x2 r=__builtin_convertvector(v,bf16x2); return __builtin_bit_cast(unsigned,r); }
DI float phi(float t){
  float e=__builtin_amdgcn_exp2f(__builtin_fminf(t,0.f)*1.44269504088896340736f);
  return e+(__builtin_fmaxf(t,0.f)+1e-6f);
}
DI bf16x8 lds128(ldscp p){ return *(const __attribute__((address_space(3))) bf16x8*)p; }
DI s16x4 ldstr(ldscp p){ return __builtin_amdgcn_ds_read_tr16_b64_v4i16((__attribute__((address_space(3))) s16x4*)p); }
DI bf16x8 cat(s16x4 lo,s16x4 hi){ return __builtin_bit_cast(bf16x8,__builtin_shufflevector(lo,hi,0,1,2,3,4,5,6,7)); }
DI void dma16(__amdgpu_buffer_rsrc_t rs,unsigned m0v,unsigned voff,unsigned soff){
  asm volatile("s_mov_b32 m0, %0\n\ts_nop 0\n\tbuffer_load_dwordx4 %1, %2, %3 offen lds"::"s"(m0v),"v"(voff),"s"(rs),"s"(soff):"memory");
}
template<int N> DI void waitbar(){ asm volatile("s_waitcnt vmcnt(%0) lgkmcnt(0)\n\ts_barrier"::"n"(N):"memory"); }
#define SB() __builtin_amdgcn_sched_barrier(0)
#define MFMA16(a,b,c) __builtin_amdgcn_mfma_f32_16x16x32_bf16((a),(b),(c),0,0,0)
#define MFMA32(a,b,c) __builtin_amdgcn_mfma_f32_32x32x16_bf16((a),(b),(c),0,0,0)

constexpr int SLOT=40960;
constexpr size_t WS_PQ=0;
constexpr size_t WS_PART=4u<<20;
constexpr size_t WS_KV=WS_PART+2*32*16*272*4;

__global__ __launch_bounds__(512,2) void k_qkv(const u16* __restrict__ x0,const u16* __restrict__ w0,const u16* __restrict__ x1,const u16* __restrict__ w1,char* __restrict__ ws){
  __shared__ __attribute__((aligned(1024))) char smem[163840];
  const int b=blockIdx.x, xcd=b&7, idx=b>>3;
  const int p=xcd>>2, q=xcd&3, r=idx&7, c=idx>>3;
  const int rt=(q&1)*8+r, cg=(q>>1)*4+c;
  const u16* x=(p?x1:x0)+(size_t)rt*128*512; const u16* w=(p?w1:w0)+cg*64;
  __amdgpu_buffer_rsrc_t rx=__builtin_amdgcn_make_buffer_rsrc((void*)x,0,128*1024,0x00020000);
  __amdgpu_buffer_rsrc_t rw=__builtin_amdgcn_make_buffer_rsrc((void*)w,0,512*3072-cg*128,0x00020000);
  const int wid=__builtin_amdgcn_readfirstlane(threadIdx.x>>6), lane=threadIdx.x&63;
  const int rot=(r&3)*2;
  const int vx=(lane>>3)*1024+(((lane&7)^(lane>>4))<<4);
  const int vw=(lane>>3)*3072+(((lane&7)^(((lane>>4)&1)<<2))<<4);
  const unsigned lbase=(unsigned)(size_t)(ldsp)smem;
  const unsigned vx0=vx, vx1=vx^64, vw0=vw^((wid&1)*32);
  auto issue1=[&](int s0,int i){
    const int s=(s0+rot)&7;
    const unsigned base=lbase+(s0&3)*SLOT;
    if(i<2){ const int t=wid*2+i; dma16(rx,base+t*1024,i?vx1:vx0,t*8192+s*128); }
    else dma16(rw,base+16384+(i-2)*8192+wid*1024,vw0,(s*64+wid*8)*3072+(i-2)*1024);
  };
  auto issue=[&](int s0){
    #pragma unroll
    for(int i=0;i<5;i++) issue1(s0,i);
  };
  const int m=wid&1, n=wid>>1;
  const int l15=lane&15, g=lane>>4;
  const int xo=(64*m+l15)*128+((g^(l15>>1))<<4);
  const int qq=l15>>2, pp=l15&3;
  const int wo=16384+(8*g+qq)*128+((((2*n+(pp>>1))^((((qq>>1)&1)<<2)|((g&1)<<1))))<<4)+(pp&1)*8;
  f32x4 aq[4],ak[4],av[4];
  #pragma unroll
  for(int t=0;t<4;t++){ aq[t]=f32x4{0,0,0,0}; ak[t]=f32x4{0,0,0,0}; av[t]=f32x4{0,0,0,0}; }
  bf16x8 xf[2][4], wf[2][3];
  auto rd1=[&](int slot,int ks,int buf,int j){
    ldscp base=(ldscp)(smem+slot*SLOT);
    if(j==0||j==2||j==3){ const int i=j==0?0:j-1; s16x4 lo=ldstr(base+wo+ks*4096+i*8192), hi=ldstr(base+wo+ks*4096+i*8192+512); wf[buf][i]=cat(lo,hi); }
    else { const int t=j==1?0:j-3; xf[buf][t]=lds128(base+((xo^(ks*64))+t*2048)); }
  };
  auto mm1=[&](int buf,int i){
    const int t=i/3, k=i%3;
    if(k==0) aq[t]=MFMA16(wf[buf][0],xf[buf][t],aq[t]);
    else if(k==1) ak[t]=MFMA16(xf[buf][t],wf[buf][1],ak[t]);
    else av[t]=MFMA16(xf[buf][t],wf[buf][2],av[t]);
  };
  issue(0);issue(1);issue(2);issue(3);
  waitbar<15>();
  #pragma unroll
  for(int j=0;j<7;j++) rd1(0,0,0,j);
  SB();
  #pragma unroll
  for(int s=0;s<8;s++){
    #pragma unroll
    for(int i=0;i<12;i++){
      mm1(0,i);
      if(i<7) rd1(s&3,1,1,i);
      SB();
    }
    if(s<7){
      if(s<=4) waitbar<10>(); else if(s==5) waitbar<5>(); else waitbar<0>();
      SB();
    }
    #pragma unroll
    for(int i=0;i<12;i++){
      mm1(1,i);
      if(s<7){
        if(i<7) rd1((s+1)&3,0,0,i);
        if(i>=7 && s+4<8) issue1(s+4,i-7);
      }
      SB();
    }
  }
  const int h=cg*4+n;
  {
    char* pq=ws+WS_PQ;
    #pragma unroll
    for(int u=0;u<2;u++){
      unsigned a0=pk2(phi(aq[2*u][0]),phi(aq[2*u][1])), a1=pk2(phi(aq[2*u][2]),phi(aq[2*u][3]));
      unsigned b0=pk2(phi(aq[2*u+1][0]),phi(aq[2*u+1][1])), b1=pk2(phi(aq[2*u+1][2]),phi(aq[2*u+1][3]));
      auto s0=__builtin_amdgcn_permlane16_swap(a0,b0,false,false);
      auto s1=__builtin_amdgcn_permlane16_swap(a1,b1,false,false);
      u32x4 v={s0[0],s1[0],s0[1],s1[1]};
      const int tb=rt*4+m*2+u;
      *(u32x4*)(pq+((size_t)((p*64+tb)*32+h))*1024+lane*16)=v;
    }
  }
  f32x4 kv={0,0,0,0}, zz={0,0,0,0};
  {
    const u32x4 ones={0x3f803f80u,0x3f803f80u,0x3f803f80u,0x3f803f80u};
    #pragma unroll
    for(int u=0;u<2;u++){
      u32x4 a={pk2(phi(ak[2*u][0]),phi(ak[2*u][1])),pk2(phi(ak[2*u][2]),phi(ak[2*u][3])),pk2(phi(ak[2*u+1][0]),phi(ak[2*u+1][1])),pk2(phi(ak[2*u+1][2]),phi(ak[2*u+1][3]))};
      u32x4 v={pk2(av[2*u][0],av[2*u][1]),pk2(av[2*u][2],av[2*u][3]),pk2(av[2*u+1][0],av[2*u+1][1]),pk2(av[2*u+1][2],av[2*u+1][3])};
      kv=MFMA16(__builtin_bit_cast(bf16x8,a),__builtin_bit_cast(bf16x8,v),kv);
      zz=MFMA16(__builtin_bit_cast(bf16x8,a),__builtin_bit_cast(bf16x8,ones),zz);
    }
  }
  __syncthreads();
  float* red=(float*)smem;
  if(m==1){
    *(f32x4*)(red+(n*2+0)*256+lane*4)=kv;
    *(f32x4*)(red+(n*2+1)*256+lane*4)=zz;
  }
  __syncthreads();
  if(m==0){
    kv+=*(f32x4*)(red+(n*2+0)*256+lane*4);
    zz+=*(f32x4*)(red+(n*2+1)*256+lane*4);
    float* part=(float*)(ws+WS_PART)+((size_t)((p*32+h)*16+rt))*272;
    *(f32x4*)(part+l15*16+4*g)=kv;
    if(l15==0) *(f32x4*)(part+256+4*g)=zz;
  }
}

__global__ __launch_bounds__(320) void k_red(char* __restrict__ ws){
  const int ph=blockIdx.x, t=threadIdx.x;
  if(t<272){
    const float* part=(const float*)(ws+WS_PART)+(size_t)ph*16*272+t;
    float a=0;
    #pragma unroll
    for(int i=0;i<16;i++) a+=part[i*272];
    __bf16 v=(__bf16)a;
    ((u16*)(ws+WS_KV))[ph*272+t]=__builtin_bit_cast(u16,v);
  }
}

__global__ __launch_bounds__(512,2) void k_out(const u16* __restrict__ wo0,const u16* __restrict__ wo1,const char* __restrict__ ws,u16* __restrict__ out){
  __shared__ __attribute__((aligned(1024))) char smem[163840];
  const int b=blockIdx.x, xcd=b&7, idx=b>>3;
  const int p=xcd>>2, q=xcd&3;
  const int cgp=idx&3, tt=q*8+(idx>>2);
  const u16* wo=(p?wo1:wo0)+cgp*128;
  __amdgpu_buffer_rsrc_t rw=__builtin_amdgcn_make_buffer_rsrc((void*)wo,0,512*1024-cgp*256,0x00020000);
  const int wid=__builtin_amdgcn_readfirstlane(threadIdx.x>>6), lane=threadIdx.x&63;
  {
    const int vw=(lane>>4)*1024+(((lane&15)^((lane>>4)<<2))<<4);
    #pragma unroll
    for(int i=0;i<16;i++){
      const int t=wid*16+i;
      dma16(rw,(unsigned)(size_t)(ldsp)smem+t*1024,vw,t*4096);
    }
  }
  const int hh=lane>>5;
  const char* pq=ws+WS_PQ; const char* kvb=ws+WS_KV+(size_t)p*32*544;
  const int kvo=(lane&16)?512+hh*16:(lane&15)*32+hh*16;
  u32x4 pf[2][2][2], kf[2][2];
  #pragma unroll
  for(int rd=0;rd<2;rd++)
    #pragma unroll
    for(int i=0;i<2;i++){
      const int h=rd*16+wid*2+i;
      kf[rd][i]=*(const u32x4*)(kvb+h*544+kvo);
      #pragma unroll
      for(int u=0;u<2;u++) pf[rd][i][u]=*(const u32x4*)(pq+((size_t)((p*64+tt*2+u)*32+h))*1024+lane*16);
    }
  auto ofrag=[&](u32x4 kvf,u32x4 pqf)->u32x4{
    f32x16 z={0,0,0,0,0,0,0,0,0,0,0,0,0,0,0,0};
    f32x16 d=MFMA32(__builtin_bit_cast(bf16x8,kvf),__builtin_bit_cast(bf16x8,pqf),z);
    float rc=__builtin_amdgcn_rcpf(d[8]+1e-6f);
    u32x4 o={pk2(d[0]*rc,d[1]*rc),pk2(d[2]*rc,d[3]*rc),pk2(d[4]*rc,d[5]*rc),pk2(d[6]*rc,d[7]*rc)};
    return o;
  };
  char* ost=smem+131072;
  const int tb=wid&1, nb=wid>>1;
  const int l15=lane&15, qq=l15>>2, pp=l15&3;
  const int ao=(4*hh+qq)*256+((((nb*4+((lane>>4)&1)*2+(pp>>1))^(qq<<2)))<<4)+(pp&1)*8;
  f32x16 acc={0,0,0,0,0,0,0,0,0,0,0,0,0,0,0,0};
  u32x4 of[2][2];
  #pragma unroll
  for(int i=0;i<2;i++)
    #pragma unroll
    for(int u=0;u<2;u++) of[i][u]=ofrag(kf[0][i],pf[0][i][u]);
  #pragma unroll
  for(int rd=0;rd<2;rd++){
    #pragma unroll
    for(int i=0;i<2;i++)
      #pragma unroll
      for(int u=0;u<2;u++) *(u32x4*)(ost+((wid*2+i)*2+u)*1024+lane*16)=of[i][u];
    waitbar<0>();
    if(rd==0){
      #pragma unroll
      for(int i=0;i<2;i++)
        #pragma unroll
        for(int u=0;u<2;u++) of[i][u]=ofrag(kf[1][i],pf[1][i][u]);
    }
    #pragma unroll
    for(int hl=0;hl<16;hl++){
      const int h=rd*16+hl;
      ldscp wb=(ldscp)(smem+h*4096+ao);
      s16x4 lo=ldstr(wb), hi=ldstr(wb+8*256);
      bf16x8 bfr=lds128((ldscp)(ost+(hl*2+tb)*1024+lane*16));
      acc=MFMA32(cat(lo,hi),bfr,acc);
    }
    __syncthreads();
  }
  {
    u16* o=out+(size_t)(tt*64+tb*32+(lane&31))*1024+p*512+cgp*128+nb*32+hh*8;
    #pragma unroll
    for(int qd=0;qd<2;qd++){
      unsigned a0=pk2(acc[8*qd+0],acc[8*qd+1]), a1=pk2(acc[8*qd+2],acc[8*qd+3]);
      unsigned b0=pk2(acc[8*qd+4],acc[8*qd+5]), b1=pk2(acc[8*qd+6],acc[8*qd+7]);
      auto s0=__builtin_amdgcn_permlane32_swap(a0,b0,false,false);
      auto s1=__builtin_amdgcn_permlane32_swap(a1,b1,false,false);
      u32x4 v={s0[0],s1[0],s0[1],s1[1]};
      *(u32x4*)(o+qd*16)=v;
    }
  }
}

extern "C" void kernel_launch(void* const* d_in, const int* in_sizes, int n_in, void* d_out, int out_size, void* d_ws, size_t ws_size, hipStream_t stream){
  const u16* x0=(const u16*)d_in[0]; const u16* w0=(const u16*)d_in[1]; const u16* wo0=(const u16*)d_in[2];
  const u16* x1=(const u16*)d_in[3]; const u16* w1=(const u16*)d_in[4]; const u16* wo1=(const u16*)d_in[5];
  char* ws=(char*)d_ws;
  k_qkv<<<256,512,0,stream>>>(x0,w0,x1,w1,ws);
  k_red<<<64,320,0,stream>>>(ws);
  k_out<<<256,512,0,stream>>>(wo0,wo1,ws,(u16*)d_out);
}
```

```cpp
#include <hip/hip_runtime.h>
typedef unsigned short u16;
typedef __attribute__((ext_vector_type(8))) __bf16 bf16x8;
typedef __attribute__((ext_vector_type(2))) __bf16 bf16x2;
typedef __attribute__((ext_vector_type(2))) float f32x2;
typedef __attribute__((ext_vector_type(4))) float f32x4;
typedef __attribute__((ext_vector_type(16))) float f32x16;
typedef __attribute__((ext_vector_type(4))) short s16x4;
typedef __attribute__((ext_vector_type(4))) unsigned u32x4;
typedef __attribute__((ext_vector_type(2))) unsigned u32x2;
typedef __attribute__((address_space(3))) void* ldsp;
typedef __attribute__((address_space(3))) const char* ldscp;
typedef __amdgpu_buffer_rsrc_t rsrc_t;
#define DI __device__ __forceinline__

DI unsigned pk2(float a,float b){ f32x2 v={a,b}; bf16x2 r=__builtin_convertvector(v,bf16x2); return __builtin_bit_cast(unsigned,r); }
DI float phi(float t){
  float e=__builtin_amdgcn_exp2f(__builtin_fminf(t,0.f)*1.44269504088896340736f);
  return e+(__builtin_fmaxf(t,0.f)+1e-6f);
}
DI bf16x8 lds128(ldscp p){ return *(const __attribute__((address_space(3))) bf16x8*)p; }
DI s16x4 ldstr(ldscp p){ return __builtin_amdgcn_ds_read_tr16_b64_v4i16((__attribute__((address_space(3))) s16x4*)p); }
DI bf16x8 cat(s16x4 lo,s16x4 hi){ return __builtin_bit_cast(bf16x8,__builtin_shufflevector(lo,hi,0,1,2,3,4,5,6,7)); }
DI void dma16(rsrc_t rs,unsigned m0v,unsigned voff,unsigned soff){
  asm volatile("s_mov_b32 m0, %0\n\ts_nop 0\n\tbuffer_load_dwordx4 %1, %2, %3 offen lds"::"s"(m0v),"v"(voff),"s"(rs),"s"(soff):"memory");
}
template<int N> DI void waitbar(){ asm volatile("s_waitcnt vmcnt(%0) lgkmcnt(0)\n\ts_barrier"::"n"(N):"memory"); }
DI void lbar(){ asm volatile("s_waitcnt lgkmcnt(0)\n\ts_barrier":::"memory"); }
DI rsrc_t mkrsrc(const void* p,unsigned bytes){
  unsigned long long a=(unsigned long long)p;
  unsigned lo=__builtin_amdgcn_readfirstlane((unsigned)a), hi=__builtin_amdgcn_readfirstlane((unsigned)(a>>32));
  return __builtin_amdgcn_make_buffer_rsrc((void*)(((unsigned long long)hi<<32)|lo),0,bytes,0x00020000);
}
#define SB() __builtin_amdgcn_sched_barrier(0)
#define MFMA16(a,b,c) __builtin_amdgcn_mfma_f32_16x16x32_bf16((a),(b),(c),0,0,0)
#define MFMA32(a,b,c) __builtin_amdgcn_mfma_f32_32x32x16_bf16((a),(b),(c),0,0,0)
#define SC1 16
#define LD16(rs,vo,so) __builtin_amdgcn_raw_buffer_load_b128((rs),(vo),(so),SC1)
#define ST16(v,rs,vo,so) __builtin_amdgcn_raw_buffer_store_b128((v),(rs),(vo),(so),SC1)

#define STAMP(i)
constexpr int SLOT=40960;
constexpr int SPIN_MAX=200000;
constexpr unsigned WS_EPOCH=0;
constexpr unsigned WS_FLAG=1024;
constexpr unsigned WS_FIN=4096;
constexpr unsigned WS_PZ=WS_FIN+2*32*68*16;
constexpr unsigned WS_PKV=WS_PZ+64*4*16*32;
constexpr unsigned WS_PQ=WS_PKV+64*4*16*512;
constexpr unsigned WS_END=WS_PQ+(4u<<20);

DI float rowsum16(float x){
  x+=__builtin_bit_cast(float,__builtin_amdgcn_update_dpp(0,__builtin_bit_cast(int,x),0x128,0xf,0xf,false));
  x+=__builtin_bit_cast(float,__builtin_amdgcn_update_dpp(0,__builtin_bit_cast(int,x),0x124,0xf,0xf,false));
  x+=__builtin_bit_cast(float,__builtin_amdgcn_update_dpp(0,__builtin_bit_cast(int,x),0x122,0xf,0xf,false));
  x+=__builtin_bit_cast(float,__builtin_amdgcn_update_dpp(0,__builtin_bit_cast(int,x),0x121,0xf,0xf,false));
  return x;
}
DI float asf(unsigned u){ return __builtin_bit_cast(float,u); }
DI unsigned asu(float f){ return __builtin_bit_cast(unsigned,f); }

__global__ __launch_bounds__(512,2) void k_fused(const u16* __restrict__ x0,const u16* __restrict__ w0,const u16* __restrict__ wo0,
                                                 const u16* __restrict__ x1,const u16* __restrict__ w1,const u16* __restrict__ wo1,
                                                 char* __restrict__ ws,u16* __restrict__ out){
  __shared__ __attribute__((aligned(1024))) char smem[163840];
  const int wid=__builtin_amdgcn_readfirstlane(threadIdx.x>>6), lane=threadIdx.x&63;
  STAMP(0);
  const int b=blockIdx.x, xcd=b&7, idx=b>>3;
  const int p=xcd>>2, q=xcd&3, r=idx&7, c=idx>>3;
  const int rt=(q&1)*8+r, cg=(q>>1)*4+c;
  const int cgp=cg>>1, tt=rt*2+(cg&1);
  const rsrc_t rx=mkrsrc((p?x1:x0)+(size_t)rt*128*512,128*1024);
  const rsrc_t rw=mkrsrc((p?w1:w0)+cg*64,512*3072-cg*128);
  const rsrc_t ro=mkrsrc((p?wo1:wo0)+cgp*128,512*1024-cgp*256);
  const rsrc_t rs=mkrsrc(ws,WS_END);
  const int rot=(r&3)*2;
  const unsigned lbase=(unsigned)(size_t)(ldsp)smem;
  const unsigned vx0=(lane>>3)*1024+(((lane&7)^(lane>>4))<<4), vx1=vx0^64;
  const unsigned vw0=((lane>>3)*3072+(((lane&7)^(((lane>>4)&1)<<2))<<4))^((wid&1)*32);
  const unsigned vo0=(lane>>4)*1024+(((lane&15)^((lane>>4)<<2))<<4);
  auto issue1=[&](int s0,int i){
    const unsigned base=lbase+(s0&3)*SLOT;
    if(s0<8){
      const int s=(s0+rot)&7;
      if(i<2){ const int t=wid*2+i; dma16(rx,base+t*1024,i?vx1:vx0,t*8192+s*128); }
      else dma16(rw,base+16384+(i-2)*8192+wid*1024,vw0,(s*64+wid*8)*3072+(i-2)*1024);
    } else if(s0<11){
      const int G=(s0-8)*40+wid*5+i; dma16(ro,lbase+G*1024,vo0,G*4096);
    } else if(i==0){
      const int G=120+wid; dma16(ro,lbase+G*1024,vo0,G*4096);
    }
  };
  #pragma unroll
  for(int s=0;s<4;s++){
    #pragma unroll
    for(int i=0;i<5;i++) issue1(s,i);
    if(s<3) __builtin_amdgcn_s_barrier();
  }
  const unsigned tag=__builtin_amdgcn_readfirstlane(__builtin_amdgcn_raw_buffer_load_b32(rs,0,WS_EPOCH+p*128,SC1))+1u;
  const int m=wid&1, n=wid>>1;
  const int l15=lane&15, g=lane>>4;
  const int xo=(64*m+l15)*128+((g^(l15>>1))<<4);
  const int qq=l15>>2, pp=l15&3;
  const int wo=16384+(8*g+qq)*128+((((2*n+(pp>>1))^((((qq>>1)&1)<<2)|((g&1)<<1))))<<4)+(pp&1)*8;
  f32x4 aq[4],ak[4],av[4];
  #pragma unroll
  for(int t=0;t<4;t++){ aq[t]=f32x4{0,0,0,0}; ak[t]=f32x4{0,0,0,0}; av[t]=f32x4{0,0,0,0}; }
  bf16x8 xf[2][4], wf[2][3];
  auto rd1=[&](int slot,int ks,int buf,int j){
    ldscp base=(ldscp)(smem+slot*SLOT);
    if(j==0||j==2||j==3){ const int i=j==0?0:j-1; s16x4 lo=ldstr(base+wo+ks*4096+i*8192), hi=ldstr(base+wo+ks*4096+i*8192+512); wf[buf][i]=cat(lo,hi); }
    else { const int t=j==1?0:j-3; xf[buf][t]=lds128(base+((xo^(ks*64))+t*2048)); }
  };
  auto mm1=[&](int buf,int i){
    const int t=i/3, k=i%3;
    if(k==0) aq[t]=MFMA16(wf[buf][0],xf[buf][t],aq[t]);
    else if(k==1) ak[t]=MFMA16(xf[buf][t],wf[buf][1],ak[t]);
    else av[t]=MFMA16(xf[buf][t],wf[buf][2],av[t]);
  };
  waitbar<15>();
  STAMP(1);
  #pragma unroll
  for(int j=0;j<7;j++) rd1(0,0,0,j);
  SB();
  #pragma unroll
  for(int s=0;s<8;s++){
    #pragma unroll
    for(int i=0;i<12;i++){
      mm1(0,i);
      if(i<7) rd1(s&3,1,1,i);
      SB();
    }
    if(s<7) waitbar<10>(); else lbar();
    SB();
    #pragma unroll
    for(int i=0;i<12;i++){
      mm1(1,i);
      if(s<7 && i<7) rd1((s+1)&3,0,0,i);
      if(i>=7) issue1(s+4,i-7);
      SB();
    }
  }
  STAMP(2);
  const int h=cg*4+n;
  f32x4 kv={0,0,0,0}, zz={0,0,0,0};
  {
    const u32x4 ones={0x3f803f80u,0x3f803f80u,0x3f803f80u,0x3f803f80u};
    #pragma unroll
    for(int u=0;u<2;u++){
      u32x4 a={pk2(phi(ak[2*u][0]),phi(ak[2*u][1])),pk2(phi(ak[2*u][2]),phi(ak[2*u][3])),pk2(phi(ak[2*u+1][0]),phi(ak[2*u+1][1])),pk2(phi(ak[2*u+1][2]),phi(ak[2*u+1][3]))};
      u32x4 v={pk2(av[2*u][0],av[2*u][1]),pk2(av[2*u][2],av[2*u][3]),pk2(av[2*u+1][0],av[2*u+1][1]),pk2(av[2*u+1][2],av[2*u+1][3])};
      kv=MFMA16(__builtin_bit_cast(bf16x8,a),__builtin_bit_cast(bf16x8,v),kv);
      zz=MFMA16(__builtin_bit_cast(bf16x8,a),__builtin_bit_cast(bf16x8,ones),zz);
    }
  }
  {
    float* red=(float*)(smem+131072);
    if(m==1){
      *(f32x4*)(red+(n*2+0)*256+lane*4)=kv;
      *(f32x4*)(red+(n*2+1)*256+lane*4)=zz;
    }
    lbar();
    if(m==0){
      kv+=*(f32x4*)(red+(n*2+0)*256+lane*4);
      zz+=*(f32x4*)(red+(n*2+1)*256+lane*4);
      const unsigned ph=p*32+h;
      const unsigned o1=WS_PKV+((ph*4+(l15>>2))*16+rt)*512+((l15&3)*8+2*g)*16;
      u32x4 g0={asu(kv[0]),tag,asu(kv[1]),tag}, g1={asu(kv[2]),tag,asu(kv[3]),tag};
      ST16(g0,rs,o1,0); ST16(g1,rs,o1+16,0);
      if(l15==0){
        const unsigned o2=WS_PZ+((ph*4+g)*16+rt)*32;
        u32x4 z0={asu(zz[0]),tag,asu(zz[1]),tag}, z1={asu(zz[2]),tag,asu(zz[3]),tag};
        ST16(z0,rs,o2,0); ST16(z1,rs,o2+16,0);
      }
    }
  }
  STAMP(3);
  {
    #pragma unroll
    for(int u=0;u<2;u++){
      unsigned a0=pk2(phi(aq[2*u][0]),phi(aq[2*u][1])), a1=pk2(phi(aq[2*u][2]),phi(aq[2*u][3]));
      unsigned b0=pk2(phi(aq[2*u+1][0]),phi(aq[2*u+1][1])), b1=pk2(phi(aq[2*u+1][2]),phi(aq[2*u+1][3]));
      auto s0=__builtin_amdgcn_permlane16_swap(a0,b0,false,false);
      auto s1=__builtin_amdgcn_permlane16_swap(a1,b1,false,false);
      u32x4 v={s0[0],s1[0],s0[1],s1[1]};
      const int tb=rt*4+m*2+u;
      ST16(v,rs,WS_PQ+((p*64+tb)*32+h)*1024+lane*16,0);
    }
  }
  waitbar<0>();
  if(wid==7 && lane==0){
    u32x2 f={tag,tag};
    __builtin_amdgcn_raw_buffer_store_b64(f,rs,WS_FLAG+((p*16+rt)*8+cg)*8,0,SC1);
  }
  STAMP(4);
  if(wid<5){
    const int hr=cg*4+(rt&3), qd=rt>>2;
    const unsigned ph=p*32+hr;
    const int prt=lane&15, gq=lane>>4;
    unsigned o1;
    if(wid<4) o1=WS_PKV+((ph*4+qd)*16+prt)*512+(wid*8+gq*2)*16;
    else      o1=WS_PZ+((ph*4+qd)*16+prt)*32;
    u32x4 a,c2;
    for(int it=0;it<SPIN_MAX;it++){
      asm volatile("":::"memory");
      a=LD16(rs,o1,0); c2=LD16(rs,o1+16,0);
      const bool ok=(a[1]==tag)&(a[3]==tag)&(c2[1]==tag)&(c2[3]==tag);
      if(__all(ok)) break;
    }
    const float s0=rowsum16(asf(a[0])), s1=rowsum16(asf(a[2])), s2=rowsum16(asf(c2[0])), s3=rowsum16(asf(c2[2]));
    u32x4 f={pk2(s0,s1),tag,pk2(s2,s3),tag};
    if(wid<4){ if(prt==0) ST16(f,rs,WS_FIN+(ph*68+qd*16+wid*4+gq)*16,0); }
    else { if(lane==0) ST16(f,rs,WS_FIN+(ph*68+64+qd)*16,0); }
  }
  STAMP(5);
  if(wid==5){
    for(int it=0;it<SPIN_MAX;it++){
      asm volatile("":::"memory");
      u32x2 f=__builtin_amdgcn_raw_buffer_load_b64(rs,WS_FLAG+((p*16+rt)*8+(lane&7))*8,0,SC1);
      if(__all((f[0]==tag)&(f[1]==tag))) break;
    }
  }
  lbar();
  STAMP(6);
  const int hh=lane>>5;
  u32x4 pf[2][2][2], kf[2][2];
  #pragma unroll
  for(int rd=0;rd<2;rd++)
    #pragma unroll
    for(int i=0;i<2;i++){
      const int hq=rd*16+wid*2+i;
      #pragma unroll
      for(int u=0;u<2;u++) pf[rd][i][u]=LD16(rs,WS_PQ+((p*64+tt*2+u)*32+hq)*1024+lane*16,0);
    }
  {
    const unsigned go=((lane&16)?64+2*hh:(lane&15)*4+2*hh)*16;
    for(int it=0;it<SPIN_MAX;it++){
      asm volatile("":::"memory");
      bool ok=true;
      #pragma unroll
      for(int rd=0;rd<2;rd++)
        #pragma unroll
        for(int i=0;i<2;i++){
          const int hq=rd*16+wid*2+i;
          const unsigned o=WS_FIN+(p*32+hq)*68*16+go;
          u32x4 a=LD16(rs,o,0), c2=LD16(rs,o+16,0);
          ok&=(a[1]==tag)&(a[3]==tag)&(c2[1]==tag)&(c2[3]==tag);
          kf[rd][i]=u32x4{a[0],a[2],c2[0],c2[2]};
        }
      if(__all(ok)) break;
    }
  }
  STAMP(7);
  if(rt==0 && cg==0 && wid==7 && lane==0) __builtin_amdgcn_raw_buffer_store_b32(tag,rs,WS_EPOCH+p*128,0,SC1);
  auto ofrag=[&](u32x4 kvf,u32x4 pqf)->u32x4{
    f32x16 z={0,0,0,0,0,0,0,0,0,0,0,0,0,0,0,0};
    f32x16 d=MFMA32(__builtin_bit_cast(bf16x8,kvf),__builtin_bit_cast(bf16x8,pqf),z);
    float rc=__builtin_amdgcn_rcpf(d[8]+1e-6f);
    u32x4 o={pk2(d[0]*rc,d[1]*rc),pk2(d[2]*rc,d[3]*rc),pk2(d[4]*rc,d[5]*rc),pk2(d[6]*rc,d[7]*rc)};
    return o;
  };
  char* ost=smem+131072;
  const int tb=wid&1, nb=wid>>1;
  const int ao=(4*hh+qq)*256+((((nb*4+((lane>>4)&1)*2+(pp>>1))^(qq<<2)))<<4)+(pp&1)*8;
  f32x16 acc={0,0,0,0,0,0,0,0,0,0,0,0,0,0,0,0};
  u32x4 of[2][2];
  #pragma unroll
  for(int i=0;i<2;i++)
    #pragma unroll
    for(int u=0;u<2;u++) of[i][u]=ofrag(kf[0][i],pf[0][i][u]);
  #pragma unroll
  for(int rd=0;rd<2;rd++){
    #pragma unroll
    for(int i=0;i<2;i++)
      #pragma unroll
      for(int u=0;u<2;u++) *(u32x4*)(ost+((wid*2+i)*2+u)*1024+lane*16)=of[i][u];
    lbar();
    if(rd==0){
      #pragma unroll
      for(int i=0;i<2;i++)
        #pragma unroll
        for(int u=0;u<2;u++) of[i][u]=ofrag(kf[1][i],pf[1][i][u]);
    }
    #pragma unroll
    for(int hl=0;hl<16;hl++){
      const int hq=rd*16+hl;
      ldscp wb=(ldscp)(smem+hq*4096+ao);
      s16x4 lo=ldstr(wb), hi=ldstr(wb+8*256);
      bf16x8 bfr=lds128((ldscp)(ost+(hl*2+tb)*1024+lane*16));
      acc=MFMA32(cat(lo,hi),bfr,acc);
    }
    if(rd==0) lbar();
  }
  STAMP(8);
  {
    u16* o=out+(size_t)(tt*64+tb*32+(lane&31))*1024+p*512+cgp*128+nb*32+hh*8;
    #pragma unroll
    for(int qd=0;qd<2;qd++){
      unsigned a0=pk2(acc[8*qd+0],acc[8*qd+1]), a1=pk2(acc[8*qd+2],acc[8*qd+3]);
      unsigned b0=pk2(acc[8*qd+4],acc[8*qd+5]), b1=pk2(acc[8*qd+6],acc[8*qd+7]);
      auto s0=__builtin_amdgcn_permlane32_swap(a0,b0,false,false);
      auto s1=__builtin_amdgcn_permlane32_swap(a1,b1,false,false);
      u32x4 v={s0[0],s1[0],s0[1],s1[1]};
      *(u32x4*)(o+qd*16)=v;
    }
  }
  STAMP(9);
}

extern "C" void kernel_launch(void* const* d_in, const int* in_sizes, int n_in, void* d_out, int out_size, void* d_ws, size_t ws_size, hipStream_t stream){
  k_fused<<<256,512,0,stream>>>((const u16*)d_in[0],(const u16*)d_in[1],(const u16*)d_in[2],(const u16*)d_in[3],(const u16*)d_in[4],(const u16*)d_in[5],(char*)d_ws,(u16*)d_out);
}
```

```cpp
#define SPIN_POINT 7
#define SPIN_T 2000ull
#include <hip/hip_runtime.h>
typedef unsigned short u16;
typedef __attribute__((ext_vector_type(8))) __bf16 bf16x8;
typedef __attribute__((ext_vector_type(2))) __bf16 bf16x2;
typedef __attribute__((ext_vector_type(2))) float f32x2;
typedef __attribute__((ext_vector_type(4))) float f32x4;
typedef __attribute__((ext_vector_type(16))) float f32x16;
typedef __attribute__((ext_vector_type(4))) short s16x4;
typedef __attribute__((ext_vector_type(4))) unsigned u32x4;
typedef __attribute__((ext_vector_type(2))) unsigned u32x2;
typedef __attribute__((address_space(3))) void* ldsp;
typedef __attribute__((address_space(3))) const char* ldscp;
typedef __amdgpu_buffer_rsrc_t rsrc_t;
#define DI __device__ __forceinline__

DI unsigned pk2(float a,float b){ f32x2 v={a,b}; bf16x2 r=__builtin_convertvector(v,bf16x2); return __builtin_bit_cast(unsigned,r); }
DI float phi(float t){
  float e=__builtin_amdgcn_exp2f(__builtin_fminf(t,0.f)*1.44269504088896340736f);
  return e+(__builtin_fmaxf(t,0.f)+1e-6f);
}
DI bf16x8 lds128(ldscp p){ return *(const __attribute__((address_space(3))) bf16x8*)p; }
DI s16x4 ldstr(ldscp p){ return __builtin_amdgcn_ds_read_tr16_b64_v4i16((__attribute__((address_space(3))) s16x4*)p); }
DI bf16x8 cat(s16x4 lo,s16x4 hi){ return __builtin_bit_cast(bf16x8,__builtin_shufflevector(lo,hi,0,1,2,3,4,5,6,7)); }
DI void dma16(rsrc_t rs,unsigned m0v,unsigned voff,unsigned soff){
  asm volatile("s_mov_b32 m0, %0\n\ts_nop 0\n\tbuffer_load_dwordx4 %1, %2, %3 offen lds"::"s"(m0v),"v"(voff),"s"(rs),"s"(soff):"memory");
}
template<int N> DI void waitbar(){ asm volatile("s_waitcnt vmcnt(%0) lgkmcnt(0)\n\ts_barrier"::"n"(N):"memory"); }
DI void lbar(){ asm volatile("s_waitcnt lgkmcnt(0)\n\ts_barrier":::"memory"); }
DI rsrc_t mkrsrc(const void* p,unsigned bytes){
  unsigned long long a=(unsigned long long)p;
  unsigned lo=__builtin_amdgcn_readfirstlane((unsigned)a), hi=__builtin_amdgcn_readfirstlane((unsigned)(a>>32));
  return __builtin_amdgcn_make_buffer_rsrc((void*)(((unsigned long long)hi<<32)|lo),0,bytes,0x00020000);
}
#define SB() __builtin_amdgcn_sched_barrier(0)
#define MFMA16(a,b,c) __builtin_amdgcn_mfma_f32_16x16x32_bf16((a),(b),(c),0,0,0)
#define MFMA32(a,b,c) __builtin_amdgcn_mfma_f32_32x32x16_bf16((a),(b),(c),0,0,0)
#define SC1 16
#define LD16(rs,vo,so) __builtin_amdgcn_raw_buffer_load_b128((rs),(vo),(so),SC1)
#define ST16(v,rs,vo,so) __builtin_amdgcn_raw_buffer_store_b128((v),(rs),(vo),(so),SC1)

#define STAMP(i) do{ if((i)==SPIN_POINT){ for(int it_=0;it_<100000;it_++){ if(__builtin_amdgcn_s_memrealtime()-tstart_>=SPIN_T) break; __builtin_amdgcn_s_sleep(1);} } }while(0)
constexpr int SLOT=40960;
constexpr int SPIN_MAX=200000;
constexpr unsigned WS_EPOCH=0;
constexpr unsigned WS_FLAG=1024;
constexpr unsigned WS_FIN=4096;
constexpr unsigned WS_PZ=WS_FIN+2*32*68*16;
constexpr unsigned WS_PKV=WS_PZ+64*4*16*32;
constexpr unsigned WS_PQ=WS_PKV+64*4*16*512;
constexpr unsigned WS_END=WS_PQ+(4u<<20);

DI float rowsum16(float x){
  x+=__builtin_bit_cast(float,__builtin_amdgcn_update_dpp(0,__builtin_bit_cast(int,x),0x128,0xf,0xf,false));
  x+=__builtin_bit_cast(float,__builtin_amdgcn_update_dpp(0,__builtin_bit_cast(int,x),0x124,0xf,0xf,false));
  x+=__builtin_bit_cast(float,__builtin_amdgcn_update_dpp(0,__builtin_bit_cast(int,x),0x122,0xf,0xf,false));
  x+=__builtin_bit_cast(float,__builtin_amdgcn_update_dpp(0,__builtin_bit_cast(int,x),0x121,0xf,0xf,false));
  return x;
}
DI float asf(unsigned u){ return __builtin_bit_cast(float,u); }
DI unsigned asu(float f){ return __builtin_bit_cast(unsigned,f); }

__global__ __launch_bounds__(512,2) void k_fused(const u16* __restrict__ x0,const u16* __restrict__ w0,const u16* __restrict__ wo0,
                                                 const u16* __restrict__ x1,const u16* __restrict__ w1,const u16* __restrict__ wo1,
                                                 char* __restrict__ ws,u16* __restrict__ out){
  __shared__ __attribute__((aligned(1024))) char smem[163840];
  const int wid=__builtin_amdgcn_readfirstlane(threadIdx.x>>6), lane=threadIdx.x&63;
  const unsigned long long tstart_=__builtin_amdgcn_s_memrealtime();
  STAMP(0);
  const int b=blockIdx.x, xcd=b&7, idx=b>>3;
  const int p=xcd>>2, q=xcd&3, r=idx&7, c=idx>>3;
  const int rt=(q&1)*8+r, cg=(q>>1)*4+c;
  const int cgp=cg>>1, tt=rt*2+(cg&1);
  const rsrc_t rx=mkrsrc((p?x1:x0)+(size_t)rt*128*512,128*1024);
  const rsrc_t rw=mkrsrc((p?w1:w0)+cg*64,512*3072-cg*128);
  const rsrc_t ro=mkrsrc((p?wo1:wo0)+cgp*128,512*1024-cgp*256);
  const rsrc_t rs=mkrsrc(ws,WS_END);
  const int rot=(r&3)*2;
  const unsigned lbase=(unsigned)(size_t)(ldsp)smem;
  const unsigned vx0=(lane>>3)*1024+(((lane&7)^(lane>>4))<<4), vx1=vx0^64;
  const unsigned vw0=((lane>>3)*3072+(((lane&7)^(((lane>>4)&1)<<2))<<4))^((wid&1)*32);
  const unsigned vo0=(lane>>4)*1024+(((lane&15)^((lane>>4)<<2))<<4);
  auto issue1=[&](int s0,int i){
    const unsigned base=lbase+(s0&3)*SLOT;
    if(s0<8){
      const int s=(s0+rot)&7;
      if(i<2){ const int t=wid*2+i; dma16(rx,base+t*1024,i?vx1:vx0,t*8192+s*128); }
      else dma16(rw,base+16384+(i-2)*8192+wid*1024,vw0,(s*64+wid*8)*3072+(i-2)*1024);
    } else if(s0<11){
      const int G=(s0-8)*40+wid*5+i; dma16(ro,lbase+G*1024,vo0,G*4096);
    } else if(i==0){
      const int G=120+wid; dma16(ro,lbase+G*1024,vo0,G*4096);
    }
  };
  #pragma unroll
  for(int s=0;s<4;s++){
    #pragma unroll
    for(int i=0;i<5;i++) issue1(s,i);
    if(s<3) __builtin_amdgcn_s_barrier();
  }
  const unsigned tag=__builtin_amdgcn_readfirstlane(__builtin_amdgcn_raw_buffer_load_b32(rs,0,WS_EPOCH+p*128,SC1))+1u;
  const int m=wid&1, n=wid>>1;
  const int l15=lane&15, g=lane>>4;
  const int xo=(64*m+l15)*128+((g^(l15>>1))<<4);
  const int qq=l15>>2, pp=l15&3;
  const int wo=16384+(8*g+qq)*128+((((2*n+(pp>>1))^((((qq>>1)&1)<<2)|((g&1)<<1))))<<4)+(pp&1)*8;
  f32x4 aq[4],ak[4],av[4];
  #pragma unroll
  for(int t=0;t<4;t++){ aq[t]=f32x4{0,0,0,0}; ak[t]=f32x4{0,0,0,0}; av[t]=f32x4{0,0,0,0}; }
  bf16x8 xf[2][4], wf[2][3];
  auto rd1=[&](int slot,int ks,int buf,int j){
    ldscp base=(ldscp)(smem+slot*SLOT);
    if(j==0||j==2||j==3){ const int i=j==0?0:j-1; s16x4 lo=ldstr(base+wo+ks*4096+i*8192), hi=ldstr(base+wo+ks*4096+i*8192+512); wf[buf][i]=cat(lo,hi); }
    else { const int t=j==1?0:j-3; xf[buf][t]=lds128(base+((xo^(ks*64))+t*2048)); }
  };
  auto mm1=[&](int buf,int i){
    const int t=i/3, k=i%3;
    if(k==0) aq[t]=MFMA16(wf[buf][0],xf[buf][t],aq[t]);
    else if(k==1) ak[t]=MFMA16(xf[buf][t],wf[buf][1],ak[t]);
    else av[t]=MFMA16(xf[buf][t],wf[buf][2],av[t]);
  };
  waitbar<15>();
  STAMP(1);
  #pragma unroll
  for(int j=0;j<7;j++) rd1(0,0,0,j);
  SB();
  #pragma unroll
  for(int s=0;s<8;s++){
    #pragma unroll
    for(int i=0;i<12;i++){
      mm1(0,i);
      if(i<7) rd1(s&3,1,1,i);
      SB();
    }
    if(s<7) waitbar<10>(); else lbar();
    SB();
    #pragma unroll
    for(int i=0;i<12;i++){
      mm1(1,i);
      if(s<7 && i<7) rd1((s+1)&3,0,0,i);
      if(i>=7) issue1(s+4,i-7);
      SB();
    }
  }
  STAMP(2);
  const int h=cg*4+n;
  f32x4 kv={0,0,0,0}, zz={0,0,0,0};
  {
    const u32x4 ones={0x3f803f80u,0x3f803f80u,0x3f803f80u,0x3f803f80u};
    #pragma unroll
    for(int u=0;u<2;u++){
      u32x4 a={pk2(phi(ak[2*u][0]),phi(ak[2*u][1])),pk2(phi(ak[2*u][2]),phi(ak[2*u][3])),pk2(phi(ak[2*u+1][0]),phi(ak[2*u+1][1])),pk2(phi(ak[2*u+1][2]),phi(ak[2*u+1][3]))};
      u32x4 v={pk2(av[2*u][0],av[2*u][1]),pk2(av[2*u][2],av[2*u][3]),pk2(av[2*u+1][0],av[2*u+1][1]),pk2(av[2*u+1][2],av[2*u+1][3])};
      kv=MFMA16(__builtin_bit_cast(bf16x8,a),__builtin_bit_cast(bf16x8,v),kv);
      zz=MFMA16(__builtin_bit_cast(bf16x8,a),__builtin_bit_cast(bf16x8,ones),zz);
    }
  }
  {
    float* red=(float*)(smem+131072);
    if(m==1){
      *(f32x4*)(red+(n*2+0)*256+lane*4)=kv;
      *(f32x4*)(red+(n*2+1)*256+lane*4)=zz;
    }
    lbar();
    if(m==0){
      kv+=*(f32x4*)(red+(n*2+0)*256+lane*4);
      zz+=*(f32x4*)(red+(n*2+1)*256+lane*4);
      const unsigned ph=p*32+h;
      const unsigned o1=WS_PKV+((ph*4+(l15>>2))*16+rt)*512+((l15&3)*8+2*g)*16;
      u32x4 g0={asu(kv[0]),tag,asu(kv[1]),tag}, g1={asu(kv[2]),tag,asu(kv[3]),tag};
      ST16(g0,rs,o1,0); ST16(g1,rs,o1+16,0);
      if(l15==0){
        const unsigned o2=WS_PZ+((ph*4+g)*16+rt)*32;
        u32x4 z0={asu(zz[0]),tag,asu(zz[1]),tag}, z1={asu(zz[2]),tag,asu(zz[3]),tag};
        ST16(z0,rs,o2,0); ST16(z1,rs,o2+16,0);
      }
    }
  }
  STAMP(3);
  {
    #pragma unroll
    for(int u=0;u<2;u++){
      unsigned a0=pk2(phi(aq[2*u][0]),phi(aq[2*u][1])), a1=pk2(phi(aq[2*u][2]),phi(aq[2*u][3]));
      unsigned b0=pk2(phi(aq[2*u+1][0]),phi(aq[2*u+1][1])), b1=pk2(phi(aq[2*u+1][2]),phi(aq[2*u+1][3]));
      auto s0=__builtin_amdgcn_permlane16_swap(a0,b0,false,false);
      auto s1=__builtin_amdgcn_permlane16_swap(a1,b1,false,false);
      u32x4 v={s0[0],s1[0],s0[1],s1[1]};
      const int tb=rt*4+m*2+u;
      ST16(v,rs,WS_PQ+((p*64+tb)*32+h)*1024+lane*16,0);
    }
  }
  waitbar<0>();
  if(wid==7 && lane==0){
    u32x2 f={tag,tag};
    __builtin_amdgcn_raw_buffer_store_b64(f,rs,WS_FLAG+((p*16+rt)*8+cg)*8,0,SC1);
  }
  STAMP(4);
  if(wid<5){
    const int hr=cg*4+(rt&3), qd=rt>>2;
    const unsigned ph=p*32+hr;
    const int prt=lane&15, gq=lane>>4;
    unsigned o1;
    if(wid<4) o1=WS_PKV+((ph*4+qd)*16+prt)*512+(wid*8+gq*2)*16;
    else      o1=WS_PZ+((ph*4+qd)*16+prt)*32;
    u32x4 a,c2;
    for(int it=0;it<SPIN_MAX;it++){
      asm volatile("":::"memory");
      a=LD16(rs,o1,0); c2=LD16(rs,o1+16,0);
      const bool ok=(a[1]==tag)&(a[3]==tag)&(c2[1]==tag)&(c2[3]==tag);
      if(__all(ok)) break;
    }
    const float s0=rowsum16(asf(a[0])), s1=rowsum16(asf(a[2])), s2=rowsum16(asf(c2[0])), s3=rowsum16(asf(c2[2]));
    u32x4 f={pk2(s0,s1),tag,pk2(s2,s3),tag};
    if(wid<4){ if(prt==0) ST16(f,rs,WS_FIN+(ph*68+qd*16+wid*4+gq)*16,0); }
    else { if(lane==0) ST16(f,rs,WS_FIN+(ph*68+64+qd)*16,0); }
  }
  STAMP(5);
  if(wid==5){
    for(int it=0;it<SPIN_MAX;it++){
      asm volatile("":::"memory");
      u32x2 f=__builtin_amdgcn_raw_buffer_load_b64(rs,WS_FLAG+((p*16+rt)*8+(lane&7))*8,0,SC1);
      if(__all((f[0]==tag)&(f[1]==tag))) break;
    }
  }
  lbar();
  STAMP(6);
  const int hh=lane>>5;
  u32x4 pf[2][2][2], kf[2][2];
  #pragma unroll
  for(int rd=0;rd<2;rd++)
    #pragma unroll
    for(int i=0;i<2;i++){
      const int hq=rd*16+wid*2+i;
      #pragma unroll
      for(int u=0;u<2;u++) pf[rd][i][u]=LD16(rs,WS_PQ+((p*64+tt*2+u)*32+hq)*1024+lane*16,0);
    }
  {
    const unsigned go=((lane&16)?64+2*hh:(lane&15)*4+2*hh)*16;
    for(int it=0;it<SPIN_MAX;it++){
      asm volatile("":::"memory");
      bool ok=true;
      #pragma unroll
      for(int rd=0;rd<2;rd++)
        #pragma unroll
        for(int i=0;i<2;i++){
          const int hq=rd*16+wid*2+i;
          const unsigned o=WS_FIN+(p*32+hq)*68*16+go;
          u32x4 a=LD16(rs,o,0), c2=LD16(rs,o+16,0);
          ok&=(a[1]==tag)&(a[3]==tag)&(c2[1]==tag)&(c2[3]==tag);
          kf[rd][i]=u32x4{a[0],a[2],c2[0],c2[2]};
        }
      if(__all(ok)) break;
    }
  }
  STAMP(7);
  if(rt==0 && cg==0 && wid==7 && lane==0) __builtin_amdgcn_raw_buffer_store_b32(tag,rs,WS_EPOCH+p*128,0,SC1);
  auto ofrag=[&](u32x4 kvf,u32x4 pqf)->u32x4{
    f32x16 z={0,0,0,0,0,0,0,0,0,0,0,0,0,0,0,0};
    f32x16 d=MFMA32(__builtin_bit_cast(bf16x8,kvf),__builtin_bit_cast(bf16x8,pqf),z);
    float rc=__builtin_amdgcn_rcpf(d[8]+1e-6f);
    u32x4 o={pk2(d[0]*rc,d[1]*rc),pk2(d[2]*rc,d[3]*rc),pk2(d[4]*rc,d[5]*rc),pk2(d[6]*rc,d[7]*rc)};
    return o;
  };
  char* ost=smem+131072;
  const int tb=wid&1, nb=wid>>1;
  const int ao=(4*hh+qq)*256+((((nb*4+((lane>>4)&1)*2+(pp>>1))^(qq<<2)))<<4)+(pp&1)*8;
  f32x16 acc={0,0,0,0,0,0,0,0,0,0,0,0,0,0,0,0};
  u32x4 of[2][2];
  #pragma unroll
  for(int i=0;i<2;i++)
    #pragma unroll
    for(int u=0;u<2;u++) of[i][u]=ofrag(kf[0][i],pf[0][i][u]);
  #pragma unroll
  for(int rd=0;rd<2;rd++){
    #pragma unroll
    for(int i=0;i<2;i++)
      #pragma unroll
      for(int u=0;u<2;u++) *(u32x4*)(ost+((wid*2+i)*2+u)*1024+lane*16)=of[i][u];
    lbar();
    if(rd==0){
      #pragma unroll
      for(int i=0;i<2;i++)
        #pragma unroll
        for(int u=0;u<2;u++) of[i][u]=ofrag(kf[1][i],pf[1][i][u]);
    }
    #pragma unroll
    for(int hl=0;hl<16;hl++){
      const int hq=rd*16+hl;
      ldscp wb=(ldscp)(smem+hq*4096+ao);
      s16x4 lo=ldstr(wb), hi=ldstr(wb+8*256);
      bf16x8 bfr=lds128((ldscp)(ost+(hl*2+tb)*1024+lane*16));
      acc=MFMA32(cat(lo,hi),bfr,acc);
    }
    if(rd==0) lbar();
  }
  STAMP(8);
  {
    u16* o=out+(size_t)(tt*64+tb*32+(lane&31))*1024+p*512+cgp*128+nb*32+hh*8;
    #pragma unroll
    for(int qd=0;qd<2;qd++){
      unsigned a0=pk2(acc[8*qd+0],acc[8*qd+1]), a1=pk2(acc[8*qd+2],acc[8*qd+3]);
      unsigned b0=pk2(acc[8*qd+4],acc[8*qd+5]), b1=pk2(acc[8*qd+6],acc[8*qd+7]);
      auto s0=__builtin_amdgcn_permlane32_swap(a0,b0,false,false);
      auto s1=__builtin_amdgcn_permlane32_swap(a1,b1,false,false);
      u32x4 v={s0[0],s1[0],s0[1],s1[1]};
      *(u32x4*)(o+qd*16)=v;
    }
  }
  STAMP(9);
}

extern "C" void kernel_launch(void* const* d_in, const int* in_sizes, int n_in, void* d_out, int out_size, void* d_ws, size_t ws_size, hipStream_t stream){
  k_fused<<<256,512,0,stream>>>((const u16*)d_in[0],(const u16*)d_in[1],(const u16*)d_in[2],(const u16*)d_in[3],(const u16*)d_in[4],(const u16*)d_in[5],(char*)d_ws,(u16*)d_out);
}
```

```cpp
#define SPIN_POINT 9
#define SPIN_T 4000ull
#include <hip/hip_runtime.h>
typedef unsigned short u16;
typedef __attribute__((ext_vector_type(8))) __bf16 bf16x8;
typedef __attribute__((ext_vector_type(2))) __bf16 bf16x2;
typedef __attribute__((ext_vector_type(2))) float f32x2;
typedef __attribute__((ext_vector_type(4))) float f32x4;
typedef __attribute__((ext_vector_type(16))) float f32x16;
typedef __attribute__((ext_vector_type(4))) short s16x4;
typedef __attribute__((ext_vector_type(4))) unsigned u32x4;
typedef __attribute__((ext_vector_type(2))) unsigned u32x2;
typedef __attribute__((address_space(3))) void* ldsp;
typedef __attribute__((address_space(3))) const char* ldscp;
typedef __amdgpu_buffer_rsrc_t rsrc_t;
#define DI __device__ __forceinline__

DI unsigned pk2(float a,float b){ f32x2 v={a,b}; bf16x2 r=__builtin_convertvector(v,bf16x2); return __builtin_bit_cast(unsigned,r); }
DI float phi(float t){
  float e=__builtin_amdgcn_exp2f(__builtin_fminf(t,0.f)*1.44269504088896340736f);
  return e+(__builtin_fmaxf(t,0.f)+1e-6f);
}
DI bf16x8 lds128(ldscp p){ return *(const __attribute__((address_space(3))) bf16x8*)p; }
DI s16x4 ldstr(ldscp p){ return __builtin_amdgcn_ds_read_tr16_b64_v4i16((__attribute__((address_space(3))) s16x4*)p); }
DI bf16x8 cat(s16x4 lo,s16x4 hi){ return __builtin_bit_cast(bf16x8,__builtin_shufflevector(lo,hi,0,1,2,3,4,5,6,7)); }
DI void dma16(rsrc_t rs,unsigned m0v,unsigned voff,unsigned soff){
  asm volatile("s_mov_b32 m0, %0\n\ts_nop 0\n\tbuffer_load_dwordx4 %1, %2, %3 offen lds"::"s"(m0v),"v"(voff),"s"(rs),"s"(soff):"memory");
}
template<int N> DI void waitbar(){ asm volatile("s_waitcnt vmcnt(%0) lgkmcnt(0)\n\ts_barrier"::"n"(N):"memory"); }
DI void lbar(){ asm volatile("s_waitcnt lgkmcnt(0)\n\ts_barrier":::"memory"); }
DI rsrc_t mkrsrc(const void* p,unsigned bytes){
  unsigned long long a=(unsigned long long)p;
  unsigned lo=__builtin_amdgcn_readfirstlane((unsigned)a), hi=__builtin_amdgcn_readfirstlane((unsigned)(a>>32));
  return __builtin_amdgcn_make_buffer_rsrc((void*)(((unsigned long long)hi<<32)|lo),0,bytes,0x00020000);
}
#define SB() __builtin_amdgcn_sched_barrier(0)
#define MFMA16(a,b,c) __builtin_amdgcn_mfma_f32_16x16x32_bf16((a),(b),(c),0,0,0)
#define MFMA32(a,b,c) __builtin_amdgcn_mfma_f32_32x32x16_bf16((a),(b),(c),0,0,0)
#define SC1 16
#define LD16(rs,vo,so) __builtin_amdgcn_raw_buffer_load_b128((rs),(vo),(so),SC1)
#define ST16(v,rs,vo,so) __builtin_amdgcn_raw_buffer_store_b128((v),(rs),(vo),(so),SC1)

#define STAMP(i) do{ if((i)==SPIN_POINT){ for(int it_=0;it_<100000;it_++){ if(__builtin_amdgcn_s_memrealtime()-tstart_>=SPIN_T) break; __builtin_amdgcn_s_sleep(1);} } }while(0)
constexpr int SLOT=40960;
constexpr int SPIN_MAX=200000;
constexpr unsigned WS_EPOCH=0;
constexpr unsigned WS_FLAG=1024;
constexpr unsigned WS_FIN=4096;
constexpr unsigned WS_PZ=WS_FIN+2*32*68*16;
constexpr unsigned WS_PKV=WS_PZ+64*4*16*32;
constexpr unsigned WS_PQ=WS_PKV+64*4*16*512;
constexpr unsigned WS_END=WS_PQ+(4u<<20);

DI float rowsum16(float x){
  x+=__builtin_bit_cast(float,__builtin_amdgcn_update_dpp(0,__builtin_bit_cast(int,x),0x128,0xf,0xf,false));
  x+=__builtin_bit_cast(float,__builtin_amdgcn_update_dpp(0,__builtin_bit_cast(int,x),0x124,0xf,0xf,false));
  x+=__builtin_bit_cast(float,__builtin_amdgcn_update_dpp(0,__builtin_bit_cast(int,x),0x122,0xf,0xf,false));
  x+=__builtin_bit_cast(float,__builtin_amdgcn_update_dpp(0,__builtin_bit_cast(int,x),0x121,0xf,0xf,false));
  return x;
}
DI float asf(unsigned u){ return __builtin_bit_cast(float,u); }
DI unsigned asu(float f){ return __builtin_bit_cast(unsigned,f); }

__global__ __launch_bounds__(512,2) void k_fused(const u16* x0_,const u16* w0_,const u16* wo0_,
                                                 const u16* x1_,const u16* w1_,const u16* wo1_,
                                                 char* ws_,u16* out_){
  unsigned long long tstart_; asm volatile("s_memrealtime %0\n\ts_waitcnt lgkmcnt(0)":"=s"(tstart_)::"memory");
  typedef __attribute__((address_space(4))) const unsigned long long* kargp;
  kargp ka=(kargp)__builtin_amdgcn_kernarg_segment_ptr();
  asm volatile("":"+s"(ka));
  const u16* x0=(const u16*)ka[0]; const u16* w0=(const u16*)ka[1]; const u16* wo0=(const u16*)ka[2];
  const u16* x1=(const u16*)ka[3]; const u16* w1=(const u16*)ka[4]; const u16* wo1=(const u16*)ka[5];
  char* ws=(char*)ka[6]; u16* out=(u16*)ka[7];
  __shared__ __attribute__((aligned(1024))) char smem[163840];
  const int wid=__builtin_amdgcn_readfirstlane(threadIdx.x>>6), lane=threadIdx.x&63;
  STAMP(0);
  const int b=blockIdx.x, xcd=b&7, idx=b>>3;
  const int p=xcd>>2, q=xcd&3, r=idx&7, c=idx>>3;
  const int rt=(q&1)*8+r, cg=(q>>1)*4+c;
  const int cgp=cg>>1, tt=rt*2+(cg&1);
  const rsrc_t rx=mkrsrc((p?x1:x0)+(size_t)rt*128*512,128*1024);
  const rsrc_t rw=mkrsrc((p?w1:w0)+cg*64,512*3072-cg*128);
  const rsrc_t ro=mkrsrc((p?wo1:wo0)+cgp*128,512*1024-cgp*256);
  const rsrc_t rs=mkrsrc(ws,WS_END);
  const rsrc_t rout=mkrsrc(out,2048*1024*2);
  const int rot=(r&3)*2;
  const unsigned lbase=(unsigned)(size_t)(ldsp)smem;
  const unsigned vx0=(lane>>3)*1024+(((lane&7)^(lane>>4))<<4), vx1=vx0^64;
  const unsigned vw0=((lane>>3)*3072+(((lane&7)^(((lane>>4)&1)<<2))<<4))^((wid&1)*32);
  const unsigned vo0=(lane>>4)*1024+(((lane&15)^((lane>>4)<<2))<<4);
  auto issue1=[&](int s0,int i){
    const unsigned base=lbase+(s0&3)*SLOT;
    if(s0<8){
      const int s=(s0+rot)&7;
      if(i<2){ const int t=wid*2+i; dma16(rx,base+t*1024,i?vx1:vx0,t*8192+s*128); }
      else dma16(rw,base+16384+(i-2)*8192+wid*1024,vw0,(s*64+wid*8)*3072+(i-2)*1024);
    } else if(s0<11){
      const int G=(s0-8)*40+wid*5+i; dma16(ro,lbase+G*1024,vo0,G*4096);
    } else if(i==0){
      const int G=120+wid; dma16(ro,lbase+G*1024,vo0,G*4096);
    }
  };
  #pragma unroll
  for(int s=0;s<4;s++){
    #pragma unroll
    for(int i=0;i<5;i++) issue1(s,i);
    if(s<3) __builtin_amdgcn_s_barrier();
  }
  const unsigned tag=__builtin_amdgcn_readfirstlane(__builtin_amdgcn_raw_buffer_load_b32(rs,0,WS_EPOCH+p*128,SC1))+1u;
  const int m=wid&1, n=wid>>1;
  const int l15=lane&15, g=lane>>4;
  const int xo=(64*m+l15)*128+((g^(l15>>1))<<4);
  const int qq=l15>>2, pp=l15&3;
  const int wo=16384+(8*g+qq)*128+((((2*n+(pp>>1))^((((qq>>1)&1)<<2)|((g&1)<<1))))<<4)+(pp&1)*8;
  f32x4 aq[4],ak[4],av[4];
  #pragma unroll
  for(int t=0;t<4;t++){ aq[t]=f32x4{0,0,0,0}; ak[t]=f32x4{0,0,0,0}; av[t]=f32x4{0,0,0,0}; }
  bf16x8 xf[2][4], wf[2][3];
  auto rd1=[&](int slot,int ks,int buf,int j){
    ldscp base=(ldscp)(smem+slot*SLOT);
    if(j==0||j==2||j==3){ const int i=j==0?0:j-1; s16x4 lo=ldstr(base+wo+ks*4096+i*8192), hi=ldstr(base+wo+ks*4096+i*8192+512); wf[buf][i]=cat(lo,hi); }
    else { const int t=j==1?0:j-3; xf[buf][t]=lds128(base+((xo^(ks*64))+t*2048)); }
  };
  auto mm1=[&](int buf,int i){
    const int t=i/3, k=i%3;
    if(k==0) aq[t]=MFMA16(wf[buf][0],xf[buf][t],aq[t]);
    else if(k==1) ak[t]=MFMA16(xf[buf][t],wf[buf][1],ak[t]);
    else av[t]=MFMA16(xf[buf][t],wf[buf][2],av[t]);
  };
  waitbar<15>();
  STAMP(1);
  #pragma unroll
  for(int j=0;j<7;j++) rd1(0,0,0,j);
  SB();
  #pragma unroll
  for(int s=0;s<8;s++){
    #pragma unroll
    for(int i=0;i<12;i++){
      mm1(0,i);
      if(i<7) rd1(s&3,1,1,i);
      SB();
    }
    if(s<7) waitbar<10>(); else lbar();
    SB();
    #pragma unroll
    for(int i=0;i<12;i++){
      mm1(1,i);
      if(s<7 && i<7) rd1((s+1)&3,0,0,i);
      if(i>=7) issue1(s+4,i-7);
      SB();
    }
  }
  STAMP(2);
  const int h=cg*4+n;
  f32x4 kv={0,0,0,0}, zz={0,0,0,0};
  {
    const u32x4 ones={0x3f803f80u,0x3f803f80u,0x3f803f80u,0x3f803f80u};
    #pragma unroll
    for(int u=0;u<2;u++){
      u32x4 a={pk2(phi(ak[2*u][0]),phi(ak[2*u][1])),pk2(phi(ak[2*u][2]),phi(ak[2*u][3])),pk2(phi(ak[2*u+1][0]),phi(ak[2*u+1][1])),pk2(phi(ak[2*u+1][2]),phi(ak[2*u+1][3]))};
      u32x4 v={pk2(av[2*u][0],av[2*u][1]),pk2(av[2*u][2],av[2*u][3]),pk2(av[2*u+1][0],av[2*u+1][1]),pk2(av[2*u+1][2],av[2*u+1][3])};
      kv=MFMA16(__builtin_bit_cast(bf16x8,a),__builtin_bit_cast(bf16x8,v),kv);
      zz=MFMA16(__builtin_bit_cast(bf16x8,a),__builtin_bit_cast(bf16x8,ones),zz);
    }
  }
  {
    float* red=(float*)(smem+131072);
    if(m==1){
      *(f32x4*)(red+(n*2+0)*256+lane*4)=kv;
      *(f32x4*)(red+(n*2+1)*256+lane*4)=zz;
    }
    lbar();
    if(m==0){
      kv+=*(f32x4*)(red+(n*2+0)*256+lane*4);
      zz+=*(f32x4*)(red+(n*2+1)*256+lane*4);
      const unsigned ph=p*32+h;
      const unsigned o1=WS_PKV+((ph*4+(l15>>2))*16+rt)*512+((l15&3)*8+2*g)*16;
      u32x4 g0={asu(kv[0]),tag,asu(kv[1]),tag}, g1={asu(kv[2]),tag,asu(kv[3]),tag};
      ST16(g0,rs,o1,0); ST16(g1,rs,o1+16,0);
      if(l15==0){
        const unsigned o2=WS_PZ+((ph*4+g)*16+rt)*32;
        u32x4 z0={asu(zz[0]),tag,asu(zz[1]),tag}, z1={asu(zz[2]),tag,asu(zz[3]),tag};
        ST16(z0,rs,o2,0); ST16(z1,rs,o2+16,0);
      }
    }
  }
  STAMP(3);
  {
    #pragma unroll
    for(int u=0;u<2;u++){
      unsigned a0=pk2(phi(aq[2*u][0]),phi(aq[2*u][1])), a1=pk2(phi(aq[2*u][2]),phi(aq[2*u][3]));
      unsigned b0=pk2(phi(aq[2*u+1][0]),phi(aq[2*u+1][1])), b1=pk2(phi(aq[2*u+1][2]),phi(aq[2*u+1][3]));
      auto s0=__builtin_amdgcn_permlane16_swap(a0,b0,false,false);
      auto s1=__builtin_amdgcn_permlane16_swap(a1,b1,false,false);
      u32x4 v={s0[0],s1[0],s0[1],s1[1]};
      const int tb=rt*4+m*2+u;
      ST16(v,rs,WS_PQ+((p*64+tb)*32+h)*1024+lane*16,0);
    }
  }
  waitbar<0>();
  if(wid==7 && lane==0){
    u32x2 f={tag,tag};
    __builtin_amdgcn_raw_buffer_store_b64(f,rs,WS_FLAG+((p*16+rt)*8+cg)*8,0,SC1);
  }
  STAMP(4);
  if(wid<5){
    const int hr=cg*4+(rt&3), qd=rt>>2;
    const unsigned ph=p*32+hr;
    const int prt=lane&15, gq=lane>>4;
    unsigned o1;
    if(wid<4) o1=WS_PKV+((ph*4+qd)*16+prt)*512+(wid*8+gq*2)*16;
    else      o1=WS_PZ+((ph*4+qd)*16+prt)*32;
    u32x4 a,c2;
    for(int it=0;it<SPIN_MAX;it++){
      asm volatile("":::"memory");
      a=LD16(rs,o1,0); c2=LD16(rs,o1+16,0);
      const bool ok=(a[1]==tag)&(a[3]==tag)&(c2[1]==tag)&(c2[3]==tag);
      if(__all(ok)) break;
    }
    const float s0=rowsum16(asf(a[0])), s1=rowsum16(asf(a[2])), s2=rowsum16(asf(c2[0])), s3=rowsum16(asf(c2[2]));
    u32x4 f={pk2(s0,s1),tag,pk2(s2,s3),tag};
    if(wid<4){ if(prt==0) ST16(f,rs,WS_FIN+(ph*68+qd*16+wid*4+gq)*16,0); }
    else { if(lane==0) ST16(f,rs,WS_FIN+(ph*68+64+qd)*16,0); }
  }
  STAMP(5);
  if(wid==5){
    for(int it=0;it<SPIN_MAX;it++){
      asm volatile("":::"memory");
      u32x2 f=__builtin_amdgcn_raw_buffer_load_b64(rs,WS_FLAG+((p*16+rt)*8+(lane&7))*8,0,SC1);
      if(__all((f[0]==tag)&(f[1]==tag))) break;
    }
  }
  lbar();
  STAMP(6);
  const int hh=lane>>5;
  u32x4 pf[2][2][2], kf[2][2];
  #pragma unroll
  for(int rd=0;rd<2;rd++)
    #pragma unroll
    for(int i=0;i<2;i++){
      const int hq=rd*16+wid*2+i;
      #pragma unroll
      for(int u=0;u<2;u++) pf[rd][i][u]=LD16(rs,WS_PQ+((p*64+tt*2+u)*32+hq)*1024+lane*16,0);
    }
  {
    const unsigned go=((lane&16)?64+2*hh:(lane&15)*4+2*hh)*16;
    for(int it=0;it<SPIN_MAX;it++){
      asm volatile("":::"memory");
      bool ok=true;
      #pragma unroll
      for(int rd=0;rd<2;rd++)
        #pragma unroll
        for(int i=0;i<2;i++){
          const int hq=rd*16+wid*2+i;
          const unsigned o=WS_FIN+(p*32+hq)*68*16+go;
          u32x4 a=LD16(rs,o,0), c2=LD16(rs,o+16,0);
          ok&=(a[1]==tag)&(a[3]==tag)&(c2[1]==tag)&(c2[3]==tag);
          kf[rd][i]=u32x4{a[0],a[2],c2[0],c2[2]};
        }
      if(__all(ok)) break;
    }
  }
  STAMP(7);
  if(rt==0 && cg==0 && wid==7 && lane==0) __builtin_amdgcn_raw_buffer_store_b32(tag,rs,WS_EPOCH+p*128,0,SC1);
  auto ofrag=[&](u32x4 kvf,u32x4 pqf)->u32x4{
    f32x16 z={0,0,0,0,0,0,0,0,0,0,0,0,0,0,0,0};
    f32x16 d=MFMA32(__builtin_bit_cast(bf16x8,kvf),__builtin_bit_cast(bf16x8,pqf),z);
    float rc=__builtin_amdgcn_rcpf(d[8]+1e-6f);
    u32x4 o={pk2(d[0]*rc,d[1]*rc),pk2(d[2]*rc,d[3]*rc),pk2(d[4]*rc,d[5]*rc),pk2(d[6]*rc,d[7]*rc)};
    return o;
  };
  char* ost=smem+131072;
  const int tb=wid&1, nb=wid>>1;
  const int ao=(4*hh+qq)*256+((((nb*4+((lane>>4)&1)*2+(pp>>1))^(qq<<2)))<<4)+(pp&1)*8;
  f32x16 acc={0,0,0,0,0,0,0,0,0,0,0,0,0,0,0,0};
  u32x4 of[2][2];
  #pragma unroll
  for(int i=0;i<2;i++)
    #pragma unroll
    for(int u=0;u<2;u++) of[i][u]=ofrag(kf[0][i],pf[0][i][u]);
  #pragma unroll
  for(int rd=0;rd<2;rd++){
    #pragma unroll
    for(int i=0;i<2;i++)
      #pragma unroll
      for(int u=0;u<2;u++) *(u32x4*)(ost+((wid*2+i)*2+u)*1024+lane*16)=of[i][u];
    lbar();
    if(rd==0){
      #pragma unroll
      for(int i=0;i<2;i++)
        #pragma unroll
        for(int u=0;u<2;u++) of[i][u]=ofrag(kf[1][i],pf[1][i][u]);
    }
    #pragma unroll
    for(int hl=0;hl<16;hl++){
      const int hq=rd*16+hl;
      ldscp wb=(ldscp)(smem+hq*4096+ao);
      s16x4 lo=ldstr(wb), hi=ldstr(wb+8*256);
      bf16x8 bfr=lds128((ldscp)(ost+(hl*2+tb)*1024+lane*16));
      acc=MFMA32(cat(lo,hi),bfr,acc);
    }
    if(rd==0) lbar();
  }
  STAMP(8);
  {
    #pragma unroll
    for(int qd=0;qd<2;qd++){
      unsigned a0=pk2(acc[8*qd+0],acc[8*qd+1]), a1=pk2(acc[8*qd+2],acc[8*qd+3]);
      unsigned b0=pk2(acc[8*qd+4],acc[8*qd+5]), b1=pk2(acc[8*qd+6],acc[8*qd+7]);
      auto s0=__builtin_amdgcn_permlane32_swap(a0,b0,false,false);
      auto s1=__builtin_amdgcn_permlane32_swap(a1,b1,false,false);
      u32x4 v={s0[0],s1[0],s0[1],s1[1]};
      __builtin_amdgcn_raw_buffer_store_b128(v,rout,(unsigned)((tt*64+tb*32+(lane&31))*2048+p*1024+cgp*256+nb*64+hh*16+qd*32),0,19);
    }
  }
  STAMP(9);
}

extern "C" void kernel_launch(void* const* d_in, const int* in_sizes, int n_in, void* d_out, int out_size, void* d_ws, size_t ws_size, hipStream_t stream){
  k_fused<<<256,512,0,stream>>>((const u16*)d_in[0],(const u16*)d_in[1],(const u16*)d_in[2],(const u16*)d_in[3],(const u16*)d_in[4],(const u16*)d_in[5],(char*)d_ws,(u16*)d_out);
}
```

```cpp
#include <hip/hip_runtime.h>
typedef unsigned short u16;
typedef __attribute__((ext_vector_type(8))) __bf16 bf16x8;
typedef __attribute__((ext_vector_type(2))) __bf16 bf16x2;
typedef __attribute__((ext_vector_type(2))) float f32x2;
typedef __attribute__((ext_vector_type(4))) float f32x4;
typedef __attribute__((ext_vector_type(16))) float f32x16;
typedef __attribute__((ext_vector_type(4))) short s16x4;
typedef __attribute__((ext_vector_type(4))) unsigned u32x4;
typedef __attribute__((ext_vector_type(2))) unsigned u32x2;
typedef __attribute__((address_space(3))) void* ldsp;
typedef __attribute__((address_space(3))) const char* ldscp;
typedef __amdgpu_buffer_rsrc_t rsrc_t;
#define DI __device__ __forceinline__

DI unsigned pk2(float a,float b){ f32x2 v={a,b}; bf16x2 r=__builtin_convertvector(v,bf16x2); return __builtin_bit_cast(unsigned,r); }
DI float phi(float t){
  float e=__builtin_amdgcn_exp2f(__builtin_fminf(t,0.f)*1.44269504088896340736f);
  return e+(__builtin_fmaxf(t,0.f)+1e-6f);
}
DI bf16x8 lds128(ldscp p){ return *(const __attribute__((address_space(3))) bf16x8*)p; }
DI s16x4 ldstr(ldscp p){ return __builtin_amdgcn_ds_read_tr16_b64_v4i16((__attribute__((address_space(3))) s16x4*)p); }
DI bf16x8 cat(s16x4 lo,s16x4 hi){ return __builtin_bit_cast(bf16x8,__builtin_shufflevector(lo,hi,0,1,2,3,4,5,6,7)); }
DI void dma16(rsrc_t rs,unsigned m0v,unsigned voff,unsigned soff){
  asm volatile("s_mov_b32 m0, %0\n\ts_nop 0\n\tbuffer_load_dwordx4 %1, %2, %3 offen lds"::"s"(m0v),"v"(voff),"s"(rs),"s"(soff):"memory");
}
template<int N> DI void waitbar(){ asm volatile("s_waitcnt vmcnt(%0) lgkmcnt(0)\n\ts_barrier"::"n"(N):"memory"); }
DI void lbar(){ asm volatile("s_waitcnt lgkmcnt(0)\n\ts_barrier":::"memory"); }
DI rsrc_t mkrsrc(const void* p,unsigned bytes){
  unsigned long long a=(unsigned long long)p;
  unsigned lo=__builtin_amdgcn_readfirstlane((unsigned)a), hi=__builtin_amdgcn_readfirstlane((unsigned)(a>>32));
  return __builtin_amdgcn_make_buffer_rsrc((void*)(((unsigned long long)hi<<32)|lo),0,bytes,0x00020000);
}
#define SB() __builtin_amdgcn_sched_barrier(0)
#define MFMA16(a,b,c) __builtin_amdgcn_mfma_f32_16x16x32_bf16((a),(b),(c),0,0,0)
#define MFMA32(a,b,c) __builtin_amdgcn_mfma_f32_32x32x16_bf16((a),(b),(c),0,0,0)
#define SC1 16
#define LD16(rs,vo,so) __builtin_amdgcn_raw_buffer_load_b128((rs),(vo),(so),SC1)
#define ST16(v,rs,vo,so) __builtin_amdgcn_raw_buffer_store_b128((v),(rs),(vo),(so),SC1)

#define STAMP(i)
constexpr int SLOT=40960;
constexpr int SPIN_MAX=200000;
constexpr unsigned WS_EPOCH=0;
constexpr unsigned WS_FLAG=1024;
constexpr unsigned WS_FIN=4096;
constexpr unsigned WS_PZ=WS_FIN+2*32*68*16;
constexpr unsigned WS_PKV=WS_PZ+64*4*16*32;
constexpr unsigned WS_PQ=WS_PKV+64*4*16*512;
constexpr unsigned WS_END=WS_PQ+(4u<<20);

DI float rowsum16(float x){
  x+=__builtin_bit_cast(float,__builtin_amdgcn_update_dpp(0,__builtin_bit_cast(int,x),0x128,0xf,0xf,false));
  x+=__builtin_bit_cast(float,__builtin_amdgcn_update_dpp(0,__builtin_bit_cast(int,x),0x124,0xf,0xf,false));
  x+=__builtin_bit_cast(float,__builtin_amdgcn_update_dpp(0,__builtin_bit_cast(int,x),0x122,0xf,0xf,false));
  x+=__builtin_bit_cast(float,__builtin_amdgcn_update_dpp(0,__builtin_bit_cast(int,x),0x121,0xf,0xf,false));
  return x;
}
DI float asf(unsigned u){ return __builtin_bit_cast(float,u); }
DI unsigned asu(float f){ return __builtin_bit_cast(unsigned,f); }

__global__ __launch_bounds__(512,2) void k_fused(const u16* x0_,const u16* w0_,const u16* wo0_,
                                                 const u16* x1_,const u16* w1_,const u16* wo1_,
                                                 char* ws_,u16* out_){
  unsigned long long tstart_; asm volatile("s_memrealtime %0\n\ts_waitcnt lgkmcnt(0)":"=s"(tstart_)::"memory");
  typedef __attribute__((address_space(4))) const unsigned long long* kargp;
  kargp ka=(kargp)__builtin_amdgcn_kernarg_segment_ptr();
  asm volatile("":"+s"(ka));
  const u16* x0=(const u16*)ka[0]; const u16* w0=(const u16*)ka[1]; const u16* wo0=(const u16*)ka[2];
  const u16* x1=(const u16*)ka[3]; const u16* w1=(const u16*)ka[4]; const u16* wo1=(const u16*)ka[5];
  char* ws=(char*)ka[6]; u16* out=(u16*)ka[7];
  __shared__ __attribute__((aligned(1024))) char smem[163840];
  const int wid=__builtin_amdgcn_readfirstlane(threadIdx.x>>6), lane=threadIdx.x&63;
  STAMP(0);
  const int b=blockIdx.x, xcd=b&7, idx=b>>3;
  const int p=xcd>>2, q=xcd&3, r=idx&7, c=idx>>3;
  const int rt=(q&1)*8+r, cg=(q>>1)*4+c;
  const int cgp=cg>>1, tt=rt*2+(cg&1);
  const rsrc_t rx=mkrsrc((p?x1:x0)+(size_t)rt*128*512,128*1024);
  const rsrc_t rw=mkrsrc((p?w1:w0)+cg*64,512*3072-cg*128);
  const rsrc_t ro=mkrsrc((p?wo1:wo0)+cgp*128,512*1024-cgp*256);
  const rsrc_t rs=mkrsrc(ws,WS_END);
  const rsrc_t rout=mkrsrc(out,2048*1024*2);
  const int rot=(r&3)*2;
  const unsigned lbase=(unsigned)(size_t)(ldsp)smem;
  const unsigned vx0=(lane>>3)*1024+(((lane&7)^(lane>>4))<<4), vx1=vx0^64;
  const unsigned vw0=((lane>>3)*3072+(((lane&7)^(((lane>>4)&1)<<2))<<4))^((wid&1)*32);
  const unsigned vo0=(lane>>4)*1024+(((lane&15)^((lane>>4)<<2))<<4);
  auto issue1=[&](int s0,int i){
    const unsigned base=lbase+(s0&3)*SLOT;
    if(s0<8){
      const int s=(s0+rot)&7;
      if(i<2){ const int t=wid*2+i; dma16(rx,base+t*1024,i?vx1:vx0,t*8192+s*128); }
      else dma16(rw,base+16384+(i-2)*8192+wid*1024,vw0,(s*64+wid*8)*3072+(i-2)*1024);
    } else if(s0<11){
      const int G=(s0-8)*40+wid*5+i; dma16(ro,lbase+G*1024,vo0,G*4096);
    } else if(i==0){
      const int G=120+wid; dma16(ro,lbase+G*1024,vo0,G*4096);
    }
  };
  #pragma unroll
  for(int s=0;s<4;s++){
    #pragma unroll
    for(int i=0;i<5;i++) issue1(s,i);
    if(s<3) __builtin_amdgcn_s_barrier();
  }
  const unsigned tag=__builtin_amdgcn_readfirstlane(__builtin_amdgcn_raw_buffer_load_b32(rs,0,WS_EPOCH+p*128,SC1))+1u;
  const int m=wid&1, n=wid>>1;
  const int l15=lane&15, g=lane>>4;
  const int xo=(64*m+l15)*128+((g^(l15>>1))<<4);
  const int qq=l15>>2, pp=l15&3;
  const int wo=16384+(8*g+qq)*128+((((2*n+(pp>>1))^((((qq>>1)&1)<<2)|((g&1)<<1))))<<4)+(pp&1)*8;
  f32x4 aq[4],ak[4],av[4];
  #pragma unroll
  for(int t=0;t<4;t++){ aq[t]=f32x4{0,0,0,0}; ak[t]=f32x4{0,0,0,0}; av[t]=f32x4{0,0,0,0}; }
  bf16x8 xf[2][4], wf[2][3];
  auto rd1=[&](int slot,int ks,int buf,int j){
    ldscp base=(ldscp)(smem+slot*SLOT);
    if(j==0||j==2||j==3){ const int i=j==0?0:j-1; s16x4 lo=ldstr(base+wo+ks*4096+i*8192), hi=ldstr(base+wo+ks*4096+i*8192+512); wf[buf][i]=cat(lo,hi); }
    else { const int t=j==1?0:j-3; xf[buf][t]=lds128(base+((xo^(ks*64))+t*2048)); }
  };
  auto mm1=[&](int buf,int i){
    const int t=i/3, k=i%3;
    if(k==0) aq[t]=MFMA16(wf[buf][0],xf[buf][t],aq[t]);
    else if(k==1) ak[t]=MFMA16(xf[buf][t],wf[buf][1],ak[t]);
    else av[t]=MFMA16(xf[buf][t],wf[buf][2],av[t]);
  };
  waitbar<15>();
  STAMP(1);
  #pragma unroll
  for(int j=0;j<7;j++) rd1(0,0,0,j);
  SB();
  #pragma unroll
  for(int s=0;s<8;s++){
    #pragma unroll
    for(int i=0;i<12;i++){
      mm1(0,i);
      if(i<7) rd1(s&3,1,1,i);
      SB();
    }
    if(s<7) waitbar<10>(); else lbar();
    SB();
    #pragma unroll
    for(int i=0;i<12;i++){
      mm1(1,i);
      if(s<7 && i<7) rd1((s+1)&3,0,0,i);
      if(i>=7) issue1(s+4,i-7);
      SB();
    }
  }
  STAMP(2);
  const int h=cg*4+n;
  f32x4 kv={0,0,0,0}, zz={0,0,0,0};
  {
    const u32x4 ones={0x3f803f80u,0x3f803f80u,0x3f803f80u,0x3f803f80u};
    #pragma unroll
    for(int u=0;u<2;u++){
      u32x4 a={pk2(phi(ak[2*u][0]),phi(ak[2*u][1])),pk2(phi(ak[2*u][2]),phi(ak[2*u][3])),pk2(phi(ak[2*u+1][0]),phi(ak[2*u+1][1])),pk2(phi(ak[2*u+1][2]),phi(ak[2*u+1][3]))};
      u32x4 v={pk2(av[2*u][0],av[2*u][1]),pk2(av[2*u][2],av[2*u][3]),pk2(av[2*u+1][0],av[2*u+1][1]),pk2(av[2*u+1][2],av[2*u+1][3])};
      kv=MFMA16(__builtin_bit_cast(bf16x8,a),__builtin_bit_cast(bf16x8,v),kv);
      zz=MFMA16(__builtin_bit_cast(bf16x8,a),__builtin_bit_cast(bf16x8,ones),zz);
    }
  }
  {
    float* red=(float*)(smem+131072);
    if(m==1){
      *(f32x4*)(red+(n*2+0)*256+lane*4)=kv;
      *(f32x4*)(red+(n*2+1)*256+lane*4)=zz;
    }
    lbar();
    if(m==0){
      kv+=*(f32x4*)(red+(n*2+0)*256+lane*4);
      zz+=*(f32x4*)(red+(n*2+1)*256+lane*4);
      const unsigned ph=p*32+h;
      const unsigned o1=WS_PKV+((ph*4+(l15>>2))*16+rt)*512+((l15&3)*8+2*g)*16;
      u32x4 g0={asu(kv[0]),tag,asu(kv[1]),tag}, g1={asu(kv[2]),tag,asu(kv[3]),tag};
      ST16(g0,rs,o1,0); ST16(g1,rs,o1+16,0);
      if(l15==0){
        const unsigned o2=WS_PZ+((ph*4+g)*16+rt)*32;
        u32x4 z0={asu(zz[0]),tag,asu(zz[1]),tag}, z1={asu(zz[2]),tag,asu(zz[3]),tag};
        ST16(z0,rs,o2,0); ST16(z1,rs,o2+16,0);
      }
    }
  }
  STAMP(3);
  {
    #pragma unroll
    for(int u=0;u<2;u++){
      unsigned a0=pk2(phi(aq[2*u][0]),phi(aq[2*u][1])), a1=pk2(phi(aq[2*u][2]),phi(aq[2*u][3]));
      unsigned b0=pk2(phi(aq[2*u+1][0]),phi(aq[2*u+1][1])), b1=pk2(phi(aq[2*u+1][2]),phi(aq[2*u+1][3]));
      auto s0=__builtin_amdgcn_permlane16_swap(a0,b0,false,false);
      auto s1=__builtin_amdgcn_permlane16_swap(a1,b1,false,false);
      u32x4 v={s0[0],s1[0],s0[1],s1[1]};
      const int tb=rt*4+m*2+u;
      ST16(v,rs,WS_PQ+((p*64+tb)*32+h)*1024+lane*16,0);
    }
  }
  waitbar<0>();
  if(wid==7 && lane==0){
    u32x2 f={tag,tag};
    __builtin_amdgcn_raw_buffer_store_b64(f,rs,WS_FLAG+((p*16+rt)*8+cg)*8,0,SC1);
  }
  STAMP(4);
  if(wid<5){
    const int hr=cg*4+(rt&3), qd=rt>>2;
    const unsigned ph=p*32+hr;
    const int prt=lane&15, gq=lane>>4;
    unsigned o1;
    if(wid<4) o1=WS_PKV+((ph*4+qd)*16+prt)*512+(wid*8+gq*2)*16;
    else      o1=WS_PZ+((ph*4+qd)*16+prt)*32;
    u32x4 a,c2;
    for(int it=0;it<SPIN_MAX;it++){
      asm volatile("":::"memory");
      a=LD16(rs,o1,0); c2=LD16(rs,o1+16,0);
      const bool ok=(a[1]==tag)&(a[3]==tag)&(c2[1]==tag)&(c2[3]==tag);
      if(__all(ok)) break;
    }
    const float s0=rowsum16(asf(a[0])), s1=rowsum16(asf(a[2])), s2=rowsum16(asf(c2[0])), s3=rowsum16(asf(c2[2]));
    u32x4 f={pk2(s0,s1),tag,pk2(s2,s3),tag};
    if(wid<4){ if(prt==0) ST16(f,rs,WS_FIN+(ph*68+qd*16+wid*4+gq)*16,0); }
    else { if(lane==0) ST16(f,rs,WS_FIN+(ph*68+64+qd)*16,0); }
  }
  STAMP(5);
  if(wid==5){
    for(int it=0;it<SPIN_MAX;it++){
      asm volatile("":::"memory");
      u32x2 f=__builtin_amdgcn_raw_buffer_load_b64(rs,WS_FLAG+((p*16+rt)*8+(lane&7))*8,0,SC1);
      if(__all((f[0]==tag)&(f[1]==tag))) break;
    }
  }
  lbar();
  STAMP(6);
  const int hh=lane>>5;
  u32x4 pf[2][2][2], kf[2][2];
  #pragma unroll
  for(int rd=0;rd<2;rd++)
    #pragma unroll
    for(int i=0;i<2;i++){
      const int hq=rd*16+wid*2+i;
      #pragma unroll
      for(int u=0;u<2;u++) pf[rd][i][u]=LD16(rs,WS_PQ+((p*64+tt*2+u)*32+hq)*1024+lane*16,0);
    }
  {
    const unsigned go=((lane&16)?64+2*hh:(lane&15)*4+2*hh)*16;
    for(int it=0;it<SPIN_MAX;it++){
      asm volatile("":::"memory");
      bool ok=true;
      #pragma unroll
      for(int rd=0;rd<2;rd++)
        #pragma unroll
        for(int i=0;i<2;i++){
          const int hq=rd*16+wid*2+i;
          const unsigned o=WS_FIN+(p*32+hq)*68*16+go;
          u32x4 a=LD16(rs,o,0), c2=LD16(rs,o+16,0);
          ok&=(a[1]==tag)&(a[3]==tag)&(c2[1]==tag)&(c2[3]==tag);
          kf[rd][i]=u32x4{a[0],a[2],c2[0],c2[2]};
        }
      if(__all(ok)) break;
    }
  }
  STAMP(7);
  if(rt==0 && cg==0 && wid==7 && lane==0) __builtin_amdgcn_raw_buffer_store_b32(tag,rs,WS_EPOCH+p*128,0,SC1);
  auto ofrag=[&](u32x4 kvf,u32x4 pqf)->u32x4{
    f32x16 z={0,0,0,0,0,0,0,0,0,0,0,0,0,0,0,0};
    f32x16 d=MFMA32(__builtin_bit_cast(bf16x8,kvf),__builtin_bit_cast(bf16x8,pqf),z);
    float rc=__builtin_amdgcn_rcpf(d[8]+1e-6f);
    u32x4 o={pk2(d[0]*rc,d[1]*rc),pk2(d[2]*rc,d[3]*rc),pk2(d[4]*rc,d[5]*rc),pk2(d[6]*rc,d[7]*rc)};
    return o;
  };
  char* ost=smem+131072;
  const int tb=wid&1, nb=wid>>1;
  const int ao=(4*hh+qq)*256+((((nb*4+((lane>>4)&1)*2+(pp>>1))^(qq<<2)))<<4)+(pp&1)*8;
  f32x16 acc={0,0,0,0,0,0,0,0,0,0,0,0,0,0,0,0};
  u32x4 of[2][2];
  #pragma unroll
  for(int i=0;i<2;i++)
    #pragma unroll
    for(int u=0;u<2;u++) of[i][u]=ofrag(kf[0][i],pf[0][i][u]);
  #pragma unroll
  for(int rd=0;rd<2;rd++){
    #pragma unroll
    for(int i=0;i<2;i++)
      #pragma unroll
      for(int u=0;u<2;u++) *(u32x4*)(ost+((wid*2+i)*2+u)*1024+lane*16)=of[i][u];
    lbar();
    if(rd==0){
      #pragma unroll
      for(int i=0;i<2;i++)
        #pragma unroll
        for(int u=0;u<2;u++) of[i][u]=ofrag(kf[1][i],pf[1][i][u]);
    }
    #pragma unroll
    for(int hl=0;hl<16;hl++){
      const int hq=rd*16+hl;
      ldscp wb=(ldscp)(smem+hq*4096+ao);
      s16x4 lo=ldstr(wb), hi=ldstr(wb+8*256);
      bf16x8 bfr=lds128((ldscp)(ost+(hl*2+tb)*1024+lane*16));
      acc=MFMA32(cat(lo,hi),bfr,acc);
    }
    if(rd==0) lbar();
  }
  STAMP(8);
  {
    #pragma unroll
    for(int qd=0;qd<2;qd++){
      unsigned a0=pk2(acc[8*qd+0],acc[8*qd+1]), a1=pk2(acc[8*qd+2],acc[8*qd+3]);
      unsigned b0=pk2(acc[8*qd+4],acc[8*qd+5]), b1=pk2(acc[8*qd+6],acc[8*qd+7]);
      auto s0=__builtin_amdgcn_permlane32_swap(a0,b0,false,false);
      auto s1=__builtin_amdgcn_permlane32_swap(a1,b1,false,false);
      u32x4 v={s0[0],s1[0],s0[1],s1[1]};
      __builtin_amdgcn_raw_buffer_store_b128(v,rout,(unsigned)((tt*64+tb*32+(lane&31))*2048+p*1024+cgp*256+nb*64+hh*16+qd*32),0,16);
    }
  }
  STAMP(9);
}

extern "C" void kernel_launch(void* const* d_in, const int* in_sizes, int n_in, void* d_out, int out_size, void* d_ws, size_t ws_size, hipStream_t stream){
  k_fused<<<256,512,0,stream>>>((const u16*)d_in[0],(const u16*)d_in[1],(const u16*)d_in[2],(const u16*)d_in[3],(const u16*)d_in[4],(const u16*)d_in[5],(char*)d_ws,(u16*)d_out);
}
```

```cpp
#include <hip/hip_runtime.h>
typedef unsigned short u16;
typedef __attribute__((ext_vector_type(8))) __bf16 bf16x8;
typedef __attribute__((ext_vector_type(2))) __bf16 bf16x2;
typedef __attribute__((ext_vector_type(2))) float f32x2;
typedef __attribute__((ext_vector_type(4))) float f32x4;
typedef __attribute__((ext_vector_type(16))) float f32x16;
typedef __attribute__((ext_vector_type(4))) short s16x4;
typedef __attribute__((ext_vector_type(4))) unsigned u32x4;
typedef __attribute__((ext_vector_type(2))) unsigned u32x2;
typedef __attribute__((address_space(3))) void* ldsp;
typedef __attribute__((address_space(3))) const char* ldscp;
typedef __attribute__((address_space(3))) char* ldswp;
typedef __amdgpu_buffer_rsrc_t rsrc_t;
#define DI __device__ __forceinline__

DI unsigned pk2(float a,float b){ f32x2 v={a,b}; bf16x2 r=__builtin_convertvector(v,bf16x2); return __builtin_bit_cast(unsigned,r); }
DI float vmaxf(float a,float b){ return __builtin_amdgcn_fmed3f(a,b,3.402823466e38f); }
DI unsigned phi2(float t1a,float t1b){
  const float L=1.44269504088896340736f;
  f32x2 t={t1a,t1b};
  f32x2 a=t*f32x2{L,L}+f32x2{-L,-L};
  float e0=__builtin_amdgcn_fmed3f(__builtin_amdgcn_exp2f(a[0]),0.f,1.f), e1=__builtin_amdgcn_fmed3f(__builtin_amdgcn_exp2f(a[1]),0.f,1.f);
  f32x2 m={vmaxf(t1a,e0),vmaxf(t1b,e1)};
  m+=f32x2{1e-6f,1e-6f};
  bf16x2 r=__builtin_convertvector(m,bf16x2); return __builtin_bit_cast(unsigned,r);
}
DI bf16x8 lds128(ldscp p){ return *(const __attribute__((address_space(3))) bf16x8*)p; }
DI s16x4 ldstr(ldscp p){ return __builtin_amdgcn_ds_read_tr16_b64_v4i16((__attribute__((address_space(3))) s16x4*)p); }
DI bf16x8 cat(s16x4 lo,s16x4 hi){ return __builtin_bit_cast(bf16x8,__builtin_shufflevector(lo,hi,0,1,2,3,4,5,6,7)); }
DI void dma16(rsrc_t rs,unsigned m0v,unsigned voff,unsigned soff){
  asm volatile("s_mov_b32 m0, %0\n\ts_nop 0\n\tbuffer_load_dwordx4 %1, %2, %3 offen lds"::"s"(m0v),"v"(voff),"s"(rs),"s"(soff):"memory");
}
template<int N> DI void waitbar(){ asm volatile("s_waitcnt vmcnt(%0) lgkmcnt(0)\n\ts_barrier"::"n"(N):"memory"); }
DI void lbar(){ asm volatile("s_waitcnt lgkmcnt(0)\n\ts_barrier":::"memory"); }
DI void rbar(){ asm volatile("s_barrier":::"memory"); }
DI rsrc_t mkrsrc(const void* p,unsigned bytes){
  unsigned long long a=(unsigned long long)p;
  unsigned lo=__builtin_amdgcn_readfirstlane((unsigned)a), hi=__builtin_amdgcn_readfirstlane((unsigned)(a>>32));
  return __builtin_amdgcn_make_buffer_rsrc((void*)(((unsigned long long)hi<<32)|lo),0,bytes,0x00020000);
}
#define SB() __builtin_amdgcn_sched_barrier(0)
#define MFMA16(a,b,c) __builtin_amdgcn_mfma_f32_16x16x32_bf16((a),(b),(c),0,0,0)
#define MFMA32(a,b,c) __builtin_amdgcn_mfma_f32_32x32x16_bf16((a),(b),(c),0,0,0)
#define SC1 16
#define LD16(rs,vo,so) __builtin_amdgcn_raw_buffer_load_b128((rs),(vo),(so),SC1)
#define LD8(rs,vo,so) __builtin_amdgcn_raw_buffer_load_b64((rs),(vo),(so),SC1)
#define ST16(v,rs,vo,so) __builtin_amdgcn_raw_buffer_store_b128((v),(rs),(vo),(so),SC1)
#define ST8(v,rs,vo,so) __builtin_amdgcn_raw_buffer_store_b64((v),(rs),(vo),(so),SC1)

#define PDIST 3
#define PQG_EARLY 0
#define CLR_AT 0
#define WO_LAST 10
constexpr int SLOT=40960;
#define SPINM 200000
constexpr int SPIN_MAX=SPINM;
constexpr unsigned WS_EPOCH=0;
constexpr unsigned WS_CLR=1024;
constexpr unsigned FINH=68*16;
constexpr unsigned WS_FIN=4096;
constexpr unsigned WS_PZ=WS_FIN+64*FINH;
constexpr unsigned WS_PKV=WS_PZ+64*16*128;
constexpr unsigned WS_PQ=WS_PKV+64*16*2048;
constexpr unsigned WS_END=WS_PQ+(4u<<20);
constexpr int OST=131072;
constexpr int FINL=0;

DI float asf(unsigned u){ return __builtin_bit_cast(float,u); }
DI unsigned asu(float f){ return __builtin_bit_cast(unsigned,f); }
DI float sum_hi4(float x){
  x+=asf(__builtin_amdgcn_update_dpp(0,__builtin_bit_cast(int,x),0x124,0xf,0xf,false));
  x+=asf(__builtin_amdgcn_update_dpp(0,__builtin_bit_cast(int,x),0x128,0xf,0xf,false));
  { auto s=__builtin_amdgcn_permlane16_swap(asu(x),asu(x),false,false); x=asf(s[0])+asf(s[1]); }
  { auto s=__builtin_amdgcn_permlane32_swap(asu(x),asu(x),false,false); x=asf(s[0])+asf(s[1]); }
  return x;
}

__global__ __launch_bounds__(512,2) void k_fused(const u16* __restrict__ x0,const u16* __restrict__ w0,const u16* __restrict__ wo0,
                                                 const u16* __restrict__ x1,const u16* __restrict__ w1,const u16* __restrict__ wo1,
                                                 char* __restrict__ ws,u16* __restrict__ out){
  __shared__ __attribute__((aligned(1024))) char smem[163840];
  const int wid=__builtin_amdgcn_readfirstlane(threadIdx.x>>6), lane=threadIdx.x&63;
  const int b=blockIdx.x, xcd=b&7, idx=b>>3;
  const int p=xcd>>2, q=xcd&3, r=idx&7, c=idx>>3;
  const int rt=(q&1)*8+r, cg=(q>>1)*4+c;
  const int tt=q*8+r, cgp=c;
  const rsrc_t rs=mkrsrc(ws,WS_END);
  const unsigned tag=*(const __attribute__((address_space(4))) unsigned*)(unsigned long long)(ws+WS_EPOCH+p*128)+1u;
  const int l15=lane&15, g=lane>>4;
  const int qq=l15>>2, pp=l15&3, hh=lane>>5;
  if(wid>=4){
    const int j=wid-4;
        const rsrc_t rx=mkrsrc((p?x1:x0)+(size_t)rt*128*512,128*1024);
    const rsrc_t rw=mkrsrc((p?w1:w0)+cg*64,512*3072-cg*128);
    const rsrc_t ro=mkrsrc((p?wo1:wo0)+cgp*128,512*1024-cgp*256);
    const int rot=(r&3)*2;
    const unsigned lbase=(unsigned)(size_t)(ldsp)smem;
    const unsigned vx0=(lane>>3)*1024+(((lane&7)^(lane>>4))<<4), vx1=vx0^64;
    const unsigned vw0=(lane>>3)*3072+(((lane&7)^(((lane>>4)&1)<<2))<<4), vw1=vw0^32;
    const unsigned vo0=(lane>>4)*1024+(((lane&15)^((lane>>4)<<2))<<4);
    auto issue=[&](int s0){
      const unsigned base=lbase+(s0&3)*SLOT;
      if(s0<8){
        const int s=(s0+rot)&7;
        #pragma unroll
        for(int i=0;i<4;i++){ const int t=j*4+i; dma16(rx,base+t*1024,(i&1)?vx1:vx0,t*8192+s*128); }
        #pragma unroll
        for(int i=0;i<6;i++){ const int kb=j*2+(i&1), blk=i>>1; dma16(rw,base+16384+blk*8192+kb*1024,(i&1)?vw1:vw0,(s*64+kb*8)*3072+blk*1024); }
      } else if(s0<11){
        #pragma unroll
        for(int i=0;i<10;i++){ const int G=(s0-8)*40+j*10+i; dma16(ro,lbase+G*1024,vo0,G*4096); }
      } else {
        #pragma unroll
        for(int i=0;i<2;i++){ const int G=120+j*2+i; dma16(ro,lbase+G*1024,vo0,G*4096); }
      }
    };
    #pragma unroll
    for(int s=0;s<PDIST;s++) issue(s);
    #pragma unroll
    for(int s=0;s<8;s++){
      const int LAST=WO_LAST;
      const int I=(s+PDIST-1)<LAST?(s+PDIST-1):LAST;
      const int nst=I-s;
      if(nst==0) waitbar<0>(); else if(nst==1) waitbar<10>(); else if(nst==2) waitbar<20>(); else waitbar<30>();

      const int nx=s+PDIST;
      if(nx<=LAST) issue(nx);
    }
    lbar();
    {
      const int I7=(7+PDIST)<WO_LAST?(7+PDIST):WO_LAST;
      #pragma unroll
      for(int s=I7+1;s<=11;s++) issue(s);
    }
    {
      const int hr=cg*4+(rt&3), qd=rt>>2;
      const unsigned ph=p*32+hr;
      const int prt=lane>>2, cc=lane&3;
      const unsigned o1=prt*2048+cc*16, s1o=WS_PKV+ph*16*2048+(qd*16+4*j)*16;
      const unsigned oz=prt*128+cc*8, szo=WS_PZ+ph*16*128+qd*32;
      u32x4 a,c2; u32x2 z={0,tag};
      for(int it=0;it<SPIN_MAX;it++){
        asm volatile("":::"memory");
        a=LD16(rs,o1,s1o); c2=LD16(rs,o1,s1o+1024);
        if(j==0) z=LD8(rs,oz,szo);
        const bool ok=(a[1]==tag)&(a[3]==tag)&(c2[1]==tag)&(c2[3]==tag)&(z[1]==tag);
        if(__all(ok)) break;
      }
      const float s0=sum_hi4(asf(a[0])), s1=sum_hi4(asf(a[2])), s2=sum_hi4(asf(c2[0])), s3=sum_hi4(asf(c2[2]));
      u32x4 f={pk2(s0,s1),tag,pk2(s2,s3),tag};
      if(lane<4) ST16(f,rs,cc*16,WS_FIN+ph*FINH+(qd*16+4*j)*16);
      if(j==0){
        const float z0=sum_hi4(asf(z[0]));
        const float z1=asf(__builtin_amdgcn_update_dpp(0,__builtin_bit_cast(int,z0),0x55,0xf,0xf,false));
        const float z2=asf(__builtin_amdgcn_update_dpp(0,__builtin_bit_cast(int,z0),0xaa,0xf,0xf,false));
        const float z3=asf(__builtin_amdgcn_update_dpp(0,__builtin_bit_cast(int,z0),0xff,0xf,0xf,false));
        u32x4 fz={pk2(z0,z1),tag,pk2(z2,z3),tag};
        if(lane==0) ST16(fz,rs,0,WS_FIN+ph*FINH+(64+qd)*16);
      }
    }
        lbar();
    u32x4 fk[9];
    const unsigned fo=lane*16, fzo=((lane>>2)&7)*FINH+(64+(lane&3))*16, fs=WS_FIN+(p*32+j*8)*FINH;
    #pragma unroll
    for(int i=0;i<8;i++) fk[i]=LD16(rs,fo,fs+i*FINH);
    fk[8]=LD16(rs,fzo,fs);
    const int nb=j;
    const int ao=(4*hh+qq)*256+((((nb*4+((lane>>4)&1)*2+(pp>>1))^(qq<<2)))<<4)+(pp&1)*8;
    bf16x8 af[32];
    #pragma unroll
    for(int hq=0;hq<32;hq++){
      ldscp wb=(ldscp)(smem+hq*4096+ao);
      af[hq]=cat(ldstr(wb),ldstr(wb+8*256));
    }
    lbar();
        {
      unsigned pend=0x1ffu;
      char* const fl=smem+FINL+j*8*544;
      const int wk=l15*32+g*8, wz=((lane>>2)&7)*544+512+(lane&3)*8;
      for(int it=0;it<SPIN_MAX;it++){
        asm volatile("":::"memory");
        #pragma unroll
        for(int i=0;i<9;i++){
          if(pend&(1u<<i)){
            const u32x4 v=fk[i];
            if(__all((v[1]==tag)&(v[3]==tag))){
              pend&=~(1u<<i);
              *(u32x2*)(fl+(i<8?i*544+wk:wz))=u32x2{v[0],v[2]};
            } else fk[i]=(i<8)?LD16(rs,fo,fs+i*FINH):LD16(rs,fzo,fs);
          }
        }
        if(pend==0) break;
      }
    }
        lbar();
    f32x16 acc[2];
    #pragma unroll
    for(int i=0;i<16;i++){ acc[0][i]=0.f; acc[1][i]=0.f; }
    bf16x8 bfr[8];
    rbar();
        #pragma unroll
    for(int i=0;i<8;i++) bfr[i]=lds128((ldscp)(smem+OST+i*1024+lane*16));
    #pragma unroll
    for(int rd=1;rd<=8;rd++){
      if(rd<8) rbar();
      SB();
      #pragma unroll
      for(int i=0;i<8;i++){
        acc[i&1]=MFMA32(af[(rd-1)*4+(i>>1)],bfr[i],acc[i&1]);
        if(rd<8) bfr[i]=lds128((ldscp)(smem+OST+(rd%3)*8192+i*1024+lane*16));
        SB();
      }
    }
        if(rt==0 && cg==0 && j==0 && lane==0) __builtin_amdgcn_raw_buffer_store_b32(tag,rs,WS_EPOCH+p*128,0,SC1);
    #pragma unroll
    for(int tb=0;tb<2;tb++){
      u16* o=out+(size_t)(tt*64+tb*32+(lane&31))*1024+p*512+cgp*128+nb*32+hh*8;
      #pragma unroll
      for(int qd=0;qd<2;qd++){
        unsigned a0=pk2(acc[tb][8*qd+0],acc[tb][8*qd+1]), a1=pk2(acc[tb][8*qd+2],acc[tb][8*qd+3]);
        unsigned b0=pk2(acc[tb][8*qd+4],acc[tb][8*qd+5]), b1=pk2(acc[tb][8*qd+6],acc[tb][8*qd+7]);
        auto s0=__builtin_amdgcn_permlane32_swap(a0,b0,false,false);
        auto s1=__builtin_amdgcn_permlane32_swap(a1,b1,false,false);
        u32x4 v={s0[0],s1[0],s0[1],s1[1]};
        *(u32x4*)(o+qd*16)=v;
      }
    }
      } else {
    const int n=wid;
        const int h=cg*4+n;
    auto clear_pq=[&](){
      const u32x4 inv={0xffffffffu,0xffffffffu,0xffffffffu,0xffffffffu};
      #pragma unroll
      for(int u=0;u<4;u++) ST16(inv,rs,lane*16,WS_PQ+((p*64+rt*4+u)*32+h)*1024);
    };
    if(CLR_AT<0) clear_pq();
    const int xo=l15*128+((g^(l15>>1))<<4);
    const int wo=16384+(8*g+qq)*128+((((2*n+(pp>>1))^((((qq>>1)&1)<<2)|((g&1)<<1))))<<4)+(pp&1)*8;
    f32x4 aq[8],ak[8],av[8];
    #pragma unroll
    for(int t=0;t<8;t++){ aq[t]=f32x4{1,1,1,1}; ak[t]=f32x4{1,1,1,1}; av[t]=f32x4{0,0,0,0}; }
    bf16x8 xf[2][8], wf[2][3];
    auto rd1=[&](int slot,int ks,int buf,int i){
      ldscp base=(ldscp)(smem+slot*SLOT);
      if(i<3){ s16x4 lo=ldstr(base+wo+ks*4096+i*8192), hi=ldstr(base+wo+ks*4096+i*8192+512); wf[buf][i]=cat(lo,hi); }
      else { const int t=i-3; xf[buf][t]=lds128(base+((xo^(ks*64))+t*2048)); }
    };
    auto mm1=[&](int buf,int i){
      const int t=i/3, k=i%3;
      if(k==0) aq[t]=MFMA16(wf[buf][0],xf[buf][t],aq[t]);
      else if(k==1) ak[t]=MFMA16(xf[buf][t],wf[buf][1],ak[t]);
      else av[t]=MFMA16(xf[buf][t],wf[buf][2],av[t]);
    };
    unsigned clr=0;
    lbar();
        #pragma unroll
    for(int i=0;i<11;i++) rd1(0,0,0,i);
    SB();
    #pragma unroll
    for(int s=0;s<8;s++){
      #pragma unroll
      for(int i=0;i<24;i++){
        mm1(0,i);
        if(i<11) rd1(s&3,1,1,i);
        SB();
      }
      if(s==CLR_AT+3) waitbar<0>(); else lbar();
      if(s==CLR_AT) clear_pq();
      if(s==CLR_AT+3 && wid==0 && lane==0) __builtin_amdgcn_raw_buffer_store_b32(tag,rs,WS_CLR+((p*16+rt)*8+cg)*4,0,SC1);
      if(s==6) clr=__builtin_amdgcn_raw_buffer_load_b32(rs,(lane&7)*4,WS_CLR+(p*16+(tt>>1))*32,SC1);
      SB();
      #pragma unroll
      for(int i=0;i<24;i++){
        mm1(1,i);
        if(s<7 && i<11) rd1((s+1)&3,0,0,i);
        SB();
      }
    }
    {
      f32x4 kv={0,0,0,0}, zz={0,0,0,0};
      const u32x4 ones={0x3f803f80u,0x3f803f80u,0x3f803f80u,0x3f803f80u};
      #pragma unroll
      for(int u=0;u<4;u++){
        u32x4 a={phi2(ak[2*u][0],ak[2*u][1]),phi2(ak[2*u][2],ak[2*u][3]),phi2(ak[2*u+1][0],ak[2*u+1][1]),phi2(ak[2*u+1][2],ak[2*u+1][3])};
        u32x4 v={pk2(av[2*u][0],av[2*u][1]),pk2(av[2*u][2],av[2*u][3]),pk2(av[2*u+1][0],av[2*u+1][1]),pk2(av[2*u+1][2],av[2*u+1][3])};
        kv=MFMA16(__builtin_bit_cast(bf16x8,a),__builtin_bit_cast(bf16x8,v),kv);
        zz=MFMA16(__builtin_bit_cast(bf16x8,ones),__builtin_bit_cast(bf16x8,a),zz);
      }
      const unsigned ph=p*32+h;
      const unsigned so=WS_PKV+(ph*16+rt)*2048;
      u32x4 g0={asu(kv[0]),tag,asu(kv[1]),tag}, g1={asu(kv[2]),tag,asu(kv[3]),tag};
      ST16(g0,rs,lane*16,so); ST16(g1,rs,lane*16,so+1024);
      if(lane<16){ u32x2 z0={asu(zz[0]),tag}; ST8(z0,rs,lane*8,WS_PZ+(ph*16+rt)*128); }
    }
    SB();
    #pragma unroll
    for(int u=0;u<4;u++){
      unsigned a0=phi2(aq[2*u][0],aq[2*u][1]), a1=phi2(aq[2*u][2],aq[2*u][3]);
      unsigned b0=phi2(aq[2*u+1][0],aq[2*u+1][1]), b1=phi2(aq[2*u+1][2],aq[2*u+1][3]);
      auto s0=__builtin_amdgcn_permlane16_swap(a0,b0,false,false);
      auto s1=__builtin_amdgcn_permlane16_swap(a1,b1,false,false);
      u32x4 v={s0[0],s1[0],s0[1],s1[1]};
      ST16(v,rs,lane*16,WS_PQ+((p*64+rt*4+u)*32+h)*1024);
    }
    for(int it=0;it<SPIN_MAX;it++){
      if(__all(clr==tag)) break;
      asm volatile("":::"memory");
      clr=__builtin_amdgcn_raw_buffer_load_b32(rs,(lane&7)*4,WS_CLR+(p*16+(tt>>1))*32,SC1);
    }
    u32x4 pf[8][2];
    const unsigned pqs=WS_PQ+((p*64+tt*2)*32+n)*1024;
    auto pload=[&](int rd,int u){ return LD16(rs,lane*16,pqs+(u*32+rd*4)*1024); };
    if(PQG_EARLY){
      #pragma unroll
      for(int rd=0;rd<8;rd++){ pf[rd][0]=pload(rd,0); pf[rd][1]=pload(rd,1); }
    }
    lbar();
        if(!PQG_EARLY){
      #pragma unroll
      for(int rd=0;rd<8;rd++){ pf[rd][0]=pload(rd,0); pf[rd][1]=pload(rd,1); }
    }
    rbar();
    {
      unsigned pend=0xffffu;
      for(int it=0;it<SPIN_MAX;it++){
        asm volatile("":::"memory");
        #pragma unroll
        for(int f=0;f<16;f++){
          if(pend&(1u<<f)){
            const u32x4 v=pf[f>>1][f&1];
            if(__all((((v[0]|v[1])|(v[2]|v[3]))&0x80008000u)==0)) pend&=~(1u<<f);
            else pf[f>>1][f&1]=pload(f>>1,f&1);
          }
        }
        if(pend==0) break;
      }
    }
        rbar();
        u32x4 kf[8];
    const int ko=FINL+n*544+((lane&16)?512:l15*32)+hh*16;
    #pragma unroll
    for(int rd=0;rd<8;rd++) kf[rd]=*(const u32x4*)(smem+ko+rd*4*544);
    const f32x16 zero16={0,0,0,0,0,0,0,0,0,0,0,0,0,0,0,0};
    f32x16 d[2][2];
    d[0][0]=MFMA32(__builtin_bit_cast(bf16x8,kf[0]),__builtin_bit_cast(bf16x8,pf[0][0]),zero16);
    d[0][1]=MFMA32(__builtin_bit_cast(bf16x8,kf[0]),__builtin_bit_cast(bf16x8,pf[0][1]),zero16);
    #pragma unroll
    for(int rd=0;rd<8;rd++){
      if(rd<7){
        d[(rd+1)&1][0]=MFMA32(__builtin_bit_cast(bf16x8,kf[rd+1]),__builtin_bit_cast(bf16x8,pf[rd+1][0]),zero16);
        d[(rd+1)&1][1]=MFMA32(__builtin_bit_cast(bf16x8,kf[rd+1]),__builtin_bit_cast(bf16x8,pf[rd+1][1]),zero16);
      }
      SB();
      #pragma unroll
      for(int u=0;u<2;u++){
        const f32x16 dd=d[rd&1][u];
        float rc=__builtin_amdgcn_rcpf(dd[8]+1e-6f);
        u32x4 o={pk2(dd[0]*rc,dd[1]*rc),pk2(dd[2]*rc,dd[3]*rc),pk2(dd[4]*rc,dd[5]*rc),pk2(dd[6]*rc,dd[7]*rc)};
        *(u32x4*)(smem+OST+(rd%3)*8192+(n*2+u)*1024+lane*16)=o;
      }
      lbar();

      SB();
    }
      }
}

extern "C" void kernel_launch(void* const* d_in, const int* in_sizes, int n_in, void* d_out, int out_size, void* d_ws, size_t ws_size, hipStream_t stream){
  k_fused<<<256,512,0,stream>>>((const u16*)d_in[0],(const u16*)d_in[1],(const u16*)d_in[2],(const u16*)d_in[3],(const u16*)d_in[4],(const u16*)d_in[5],(char*)d_ws,(u16*)d_out);
}
```

```cpp
#include <hip/hip_runtime.h>
typedef unsigned short u16;
typedef __attribute__((ext_vector_type(8))) __bf16 bf16x8;
typedef __attribute__((ext_vector_type(2))) __bf16 bf16x2;
typedef __attribute__((ext_vector_type(2))) float f32x2;
typedef __attribute__((ext_vector_type(4))) float f32x4;
typedef __attribute__((ext_vector_type(16))) float f32x16;
typedef __attribute__((ext_vector_type(4))) short s16x4;
typedef __attribute__((ext_vector_type(4))) unsigned u32x4;
typedef __attribute__((ext_vector_type(2))) unsigned u32x2;
typedef __attribute__((address_space(3))) void* ldsp;
typedef __attribute__((address_space(3))) const char* ldscp;
typedef __amdgpu_buffer_rsrc_t rsrc_t;
#define DI __device__ __forceinline__

DI unsigned pk2(float a,float b){ f32x2 v={a,b}; bf16x2 r=__builtin_convertvector(v,bf16x2); return __builtin_bit_cast(unsigned,r); }
DI float vmaxf(float a,float b){ return __builtin_amdgcn_fmed3f(a,b,3.402823466e38f); }
DI unsigned phi2(float t1a,float t1b){
  const float L=1.44269504088896340736f;
  f32x2 t={t1a,t1b};
  f32x2 a=t*f32x2{L,L}+f32x2{-L,-L};
  float e0=__builtin_amdgcn_fmed3f(__builtin_amdgcn_exp2f(a[0]),0.f,1.f), e1=__builtin_amdgcn_fmed3f(__builtin_amdgcn_exp2f(a[1]),0.f,1.f);
  f32x2 m={vmaxf(t1a,e0),vmaxf(t1b,e1)};
  m+=f32x2{1e-6f,1e-6f};
  bf16x2 r=__builtin_convertvector(m,bf16x2); return __builtin_bit_cast(unsigned,r);
}
DI bf16x8 lds128(ldscp p){ return *(const __attribute__((address_space(3))) bf16x8*)p; }
DI s16x4 ldstr(ldscp p){ return __builtin_amdgcn_ds_read_tr16_b64_v4i16((__attribute__((address_space(3))) s16x4*)p); }
DI bf16x8 cat(s16x4 lo,s16x4 hi){ return __builtin_bit_cast(bf16x8,__builtin_shufflevector(lo,hi,0,1,2,3,4,5,6,7)); }
DI void dma16(rsrc_t rs,unsigned m0v,unsigned voff,unsigned soff){
  asm volatile("s_mov_b32 m0, %0\n\ts_nop 0\n\tbuffer_load_dwordx4 %1, %2, %3 offen lds"::"s"(m0v),"v"(voff),"s"(rs),"s"(soff):"memory");
}
DI void dma4(rsrc_t rs,unsigned m0v,unsigned voff,unsigned soff){
  asm volatile("s_mov_b32 m0, %0\n\ts_nop 0\n\tbuffer_load_dword %1, %2, %3 offen lds"::"s"(m0v),"v"(voff),"s"(rs),"s"(soff):"memory");
}
template<int N> DI void waitbar(){ asm volatile("s_waitcnt vmcnt(%0) lgkmcnt(0)\n\ts_barrier"::"n"(N):"memory"); }
template<int N> DI void waitvm(){ asm volatile("s_waitcnt vmcnt(%0)"::"n"(N):"memory"); }
DI void waitlg(){ asm volatile("s_waitcnt lgkmcnt(0)":::"memory"); }
DI void lbar(){ asm volatile("s_waitcnt lgkmcnt(0)\n\ts_barrier":::"memory"); }
DI void rbar(){ asm volatile("s_barrier":::"memory"); }
DI rsrc_t mkrsrc(const void* p,unsigned bytes){
  unsigned long long a=(unsigned long long)p;
  unsigned lo=__builtin_amdgcn_readfirstlane((unsigned)a), hi=__builtin_amdgcn_readfirstlane((unsigned)(a>>32));
  return __builtin_amdgcn_make_buffer_rsrc((void*)(((unsigned long long)hi<<32)|lo),0,bytes,0x00020000);
}
#define SB() __builtin_amdgcn_sched_barrier(0)
#define MFMA16(a,b,c) __builtin_amdgcn_mfma_f32_16x16x32_bf16((a),(b),(c),0,0,0)
#define MFMA32(a,b,c) __builtin_amdgcn_mfma_f32_32x32x16_bf16((a),(b),(c),0,0,0)
#define SC1 16
#define LD16(rs,vo,so) __builtin_amdgcn_raw_buffer_load_b128((rs),(vo),(so),SC1)
#define LD8(rs,vo,so) __builtin_amdgcn_raw_buffer_load_b64((rs),(vo),(so),SC1)
#define ST16(v,rs,vo,so) __builtin_amdgcn_raw_buffer_store_b128((v),(rs),(vo),(so),SC1)
#define ST8(v,rs,vo,so) __builtin_amdgcn_raw_buffer_store_b64((v),(rs),(vo),(so),SC1)

#define CPF 0
#define OUTT 1
#define HPRIO2 0
#define CPRIO2 -1
#define CPF2 0
#define PF 1
#define ROTX ((r+2*c)&7)
#define PRIO_C 2
#define PRIO_H 0
#define OUTAUX 16
#define SPINM 200000
constexpr int SPIN_MAX=SPINM;
constexpr unsigned WS_EPOCH=0;
constexpr unsigned WS_CLR=1024;
constexpr unsigned FINH=640;
constexpr unsigned WS_FIN=4096;
constexpr unsigned WS_PZ=WS_FIN+64*FINH;
constexpr unsigned WS_PKV=WS_PZ+64*16*128;
constexpr unsigned WS_PQ=WS_PKV+64*16*2048;
constexpr unsigned WS_END=WS_PQ+(4u<<20);
constexpr int RES=0;
constexpr int RING=65536;
constexpr int ASLOT=24576;

DI float asf(unsigned u){ return __builtin_bit_cast(float,u); }
DI unsigned asu(float f){ return __builtin_bit_cast(unsigned,f); }
DI float sum_hi4(float x){
  x+=asf(__builtin_amdgcn_update_dpp(0,__builtin_bit_cast(int,x),0x124,0xf,0xf,false));
  x+=asf(__builtin_amdgcn_update_dpp(0,__builtin_bit_cast(int,x),0x128,0xf,0xf,false));
  { auto s=__builtin_amdgcn_permlane16_swap(asu(x),asu(x),false,false); x=asf(s[0])+asf(s[1]); }
  { auto s=__builtin_amdgcn_permlane32_swap(asu(x),asu(x),false,false); x=asf(s[0])+asf(s[1]); }
  return x;
}
DI constexpr int wloc(int i){ return i<4 ? RING+(i+2)*16384 : (i<6 ? RING+(i-4)*16384 : RES+(i-6)*16384); }
DI constexpr int ostloc(int r){ return r<2 ? RES+32768+r*16384 : RING+(r-2)*16384; }

__global__ __launch_bounds__(512,2) void k_fused(const u16* __restrict__ x0,const u16* __restrict__ w0,const u16* __restrict__ wo0,
                                                 const u16* __restrict__ x1,const u16* __restrict__ w1,const u16* __restrict__ wo1,
                                                 char* __restrict__ ws,u16* __restrict__ out){
  __shared__ __attribute__((aligned(1024))) char smem[163840];
  const int wid=__builtin_amdgcn_readfirstlane(threadIdx.x>>6), lane=threadIdx.x&63;
  const unsigned lbase=(unsigned)(size_t)(ldsp)smem;
  SB();
  const int b=blockIdx.x, xcd=b&7, idx=b>>3;
  const int p=xcd>>2, q=xcd&3, r=idx&7, c=idx>>3;
  const int half=q>>1, rt=(q&1)*8+r, cg=half*4+c, tt=2*rt+half;
    const rsrc_t rx=mkrsrc((p?x1:x0)+(size_t)rt*128*512,128*1024);
  const rsrc_t rw=mkrsrc(p?w1:w0,512*3072);
  const int rot=ROTX;
  const unsigned vx0=(lane>>3)*1024+(((lane&7)^(lane>>4))<<4), vx1=vx0^64;
  const unsigned vw0=(lane>>3)*3072+(((lane&7)^(((lane>>4)&1)<<2))<<4), vw1=vw0^32;
  {
    const unsigned vws=vw0^((wid&1)<<5);
    #pragma unroll
    for(int s=0;s<4;s++){
      const int ka=(s+rot)&7;
      const unsigned xd=(wid<4)?(lbase+RES+s*8192+wid*2048):(lbase+RING+s*ASLOT+(wid-4)*2048);
      #pragma unroll
      for(int ii=0;ii<2;ii++) dma16(rx,xd+ii*1024,ii?vx1:vx0,(((2*wid+ii)*8)^(half<<6))*1024+ka*128);
      #pragma unroll
      for(int blk=0;blk<2;blk++) dma16(rw,lbase+RING+s*ASLOT+8192+blk*8192+wid*1024,vws,(ka*64+wid*8)*3072+(blk+1)*1024+cg*128);
    }
  }
  const rsrc_t ro=mkrsrc(p?wo1:wo0,512*1024);
  const rsrc_t rs=mkrsrc(ws,WS_END);
  if(PF && wid>=4){
    const int j=wid-4; const unsigned L=r*128+(j&1)*64+lane;
    if(j<2) dma4(rw,lbase+RES+7*8192+j*256,(L>>1)*3072+(L&1)*128,c*256);
    else    dma4(ro,lbase+RES+7*8192+j*256,(L>>1)*1024+(L&1)*128,c*256);
  }
  unsigned ep;
  { const char* ea=ws+WS_EPOCH+p*128;
    asm volatile("s_load_dword %0, %1, 0x0\n\ts_waitcnt lgkmcnt(0)":"=s"(ep):"s"(ea):"memory"); }
  const unsigned tag=ep+1u;
  const int l15=lane&15, g=lane>>4;
  const int qq=l15>>2, pp=l15&3, hh=lane>>5;
  const int xo=l15*128+((g^(l15>>1))<<4);
  if(wid>=4){
    const int j=wid-4;
    if(PRIO_H) __builtin_amdgcn_s_setprio(PRIO_H);
    const unsigned vq=(lane>>2)*3072+(((lane&3)^(((lane>>5)&1)<<1))<<4);
    auto issueA=[&](int s){
      const int ka=(s+rot)&7;
      const unsigned xd=(j<2)?(lbase+RES+s*8192+j*4096):(lbase+RING+(s&3)*ASLOT+(j-2)*4096);
      #pragma unroll
      for(int ii=0;ii<4;ii++) dma16(rx,xd+ii*1024,(ii&1)?vx1:vx0,(((4*j+ii)*8)^(half<<6))*1024+ka*128);
      #pragma unroll
      for(int ii=0;ii<4;ii++){ const int kb=2*j+(ii&1), blk=ii>>1;
        dma16(rw,lbase+RING+(s&3)*ASLOT+8192+blk*8192+kb*1024,(ii&1)?vw1:vw0,(ka*64+kb*8)*3072+(blk+1)*1024+cg*128); }
    };
    auto issueB=[&](int s){
      const int ka=(s+rot)&7, slot=s<6?s:s-6;
      #pragma unroll
      for(int ii=0;ii<4;ii++) dma16(rw,lbase+RING+slot*16384+j*4096+ii*1024,vq,(ka*64+ii*16)*3072+c*256+j*64);
    };
#define BT(i)
    waitbar<12+PF>(); BT(0);
    waitbar<8+PF>();  BT(1); issueA(4);
    waitbar<12+PF>(); BT(2); issueA(5);
    waitbar<16+PF>(); BT(3); issueA(6);
    waitbar<16>();    BT(4); issueA(7);
    waitbar<16>();    BT(5); issueB(0);
    waitbar<12>();    BT(6); issueB(1); issueB(2);
    waitbar<12>();    BT(7); issueB(3);
    rbar();           BT(8); issueB(4); issueB(5);
    f32x16 aq[2];
    #pragma unroll
    for(int i=0;i<16;i++){ aq[0][i]=1.f; aq[1][i]=1.f; }
    {
      const int a0=RING+j*4096+(8*hh+qq)*64+((((((lane>>4)&1)*2+(pp>>1))^(hh<<1)))<<4)+(pp&1)*8, a1=a0+256;
      const int l31=lane&31;
      const int xq=RES+l31*128+((hh^((l31>>1)&7))<<4);
      bf16x8 xf[2][8], wf[2][4];
      auto rdq=[&](int s,int buf,int i){
        const int ks=i/3, e=i%3;
        if(e==0){ const int o=(s<6?s:s-6)*16384+ks*1024; wf[buf][ks]=cat(ldstr((ldscp)(smem+o+a0)),ldstr((ldscp)(smem+o+a1))); }
        else { const int u=e-1; xf[buf][ks*2+u]=lds128((ldscp)(smem+s*8192+u*4096+(xq^(ks*32)))); }
      };
      waitvm<20>();
      #pragma unroll
      for(int i=0;i<12;i++) rdq(0,0,i);
      SB();
      #pragma unroll
      for(int s=0;s<8;s++){
        if(s<3) waitvm<16>(); else if(s==3) waitvm<12>(); else if(s==4) waitvm<8>(); else if(s==5) waitvm<4>(); else if(s==6) waitvm<0>();
        SB();
        #pragma unroll
        for(int i=0;i<8;i++){
          aq[i&1]=MFMA32(wf[s&1][i>>1],xf[s&1][i],aq[i&1]);
          if(s<7 && i<6){ rdq(s+1,(s+1)&1,2*i); rdq(s+1,(s+1)&1,2*i+1); }
          SB();
        }
        if(s<2) issueB(6+s);
        if(s==4 || s==7) rbar();
        SB();
      }
    }
        const u32x2 clr0=LD8(rs,lane*8,WS_CLR+p*512);
    u32x4 pq[4][2][2];
    const unsigned pqb=WS_PQ+((p*32+tt)*32+2*j)*2048;
    #pragma unroll
    for(int u=0;u<2;u++){
      #pragma unroll
      for(int hs=0;hs<2;hs++){
        u32x4 v={phi2(aq[u][8*hs+0],aq[u][8*hs+1]),phi2(aq[u][8*hs+2],aq[u][8*hs+3]),phi2(aq[u][8*hs+4],aq[u][8*hs+5]),phi2(aq[u][8*hs+6],aq[u][8*hs+7])};
        pq[0][hs][u]=v;
        ST16(v,rs,lane*16+hs*2048+u*1024,pqb+c*16384);
      }
    }
    if(!__all((clr0[0]==tag)&(clr0[1]==tag))){
      #pragma nounroll
      for(int it=0;it<SPIN_MAX;it++){
        const u32x2 cl=LD8(rs,lane*8,WS_CLR+p*512);
        if(__all((cl[0]==tag)&(cl[1]==tag))) break;
        asm volatile("":::"memory");
      }
    }
    const unsigned fo=(lane&16)?(512+hh*16):(hh*256+l15*16);
    unsigned fso[4], pso[4];
    #pragma unroll
    for(int rd=0;rd<4;rd++){ fso[rd]=WS_FIN+(p*32+((rd^c)*8)+2*j)*FINH; pso[rd]=pqb+(rd^c)*16384; asm volatile("":"+s"(fso[rd]),"+s"(pso[rd])); }
    rbar();
    if(HPRIO2) __builtin_amdgcn_s_setprio(HPRIO2);
    u32x4 fin[4][2];
    auto fload=[&](int rd,int hs){ return LD16(rs,fo+hs*FINH,fso[rd]); };
    auto pload=[&](int rd,int i){ return LD16(rs,lane*16+i*1024,pso[rd]); };
    #pragma unroll
    for(int rd=0;rd<4;rd++){
      fin[rd][0]=fload(rd,0); fin[rd][1]=fload(rd,1);
      SB();
      if(rd<3){
        #pragma unroll
        for(int i=0;i<4;i++) pq[rd+1][i>>1][i&1]=pload(rd+1,i);
      }
      SB();
    }
    auto finok=[&](const u32x4& v){ unsigned m=max(max(v[0],v[1]),max(v[2],v[3])); return m!=0xffffffffu; };
    auto valid=[&](int rd){
      bool ok=finok(fin[rd][0])&finok(fin[rd][1]);
      if(rd){ unsigned m=0;
        #pragma unroll
        for(int i=0;i<4;i++){ const u32x4 v=pq[rd][i>>1][i&1]; m|=(v[0]|v[1])|(v[2]|v[3]); }
        ok&=(m&0x80008000u)==0; }
      return (bool)__all(ok);
    };
    const f32x16 zero16={0,0,0,0,0,0,0,0,0,0,0,0,0,0,0,0};
    #pragma unroll
    for(int rd=0;rd<4;rd++){
      if(!valid(rd)){
        #pragma nounroll
        for(int it=0;it<SPIN_MAX;it++){
          asm volatile("":::"memory");
          fin[rd][0]=fload(rd,0); fin[rd][1]=fload(rd,1);
          if(rd){
            #pragma unroll
            for(int i=0;i<4;i++) pq[rd][i>>1][i&1]=pload(rd,i);
          }
          if(valid(rd)) break;
        }
      }

      SB();
      f32x16 d[2][2];
      #pragma unroll
      for(int hs=0;hs<2;hs++){
        d[hs][0]=MFMA32(__builtin_bit_cast(bf16x8,fin[rd][hs]),__builtin_bit_cast(bf16x8,pq[rd][hs][0]),zero16);
        d[hs][1]=MFMA32(__builtin_bit_cast(bf16x8,fin[rd][hs]),__builtin_bit_cast(bf16x8,pq[rd][hs][1]),zero16);
      }
      SB();
      #pragma unroll
      for(int hs=0;hs<2;hs++){
        #pragma unroll
        for(int u=0;u<2;u++){
          const f32x16 dd=d[hs][u];
          float rc=__builtin_amdgcn_rcpf(dd[8]+1e-6f);
          u32x4 o={pk2(dd[0]*rc,dd[1]*rc),pk2(dd[2]*rc,dd[3]*rc),pk2(dd[4]*rc,dd[5]*rc),pk2(dd[6]*rc,dd[7]*rc)};
          *(u32x4*)(smem+ostloc(rd)+((2*j+hs)*2+u)*1024+lane*16)=o;
        }
      }
      SB();
      lbar();
      SB();

    }
      } else {
    const int n=wid;
    if(PRIO_C) __builtin_amdgcn_s_setprio(PRIO_C);
    const int h=cg*4+n;
    const int wo=RING+8192+(8*g+qq)*128+((((2*n+(pp>>1))^((((qq>>1)&1)<<2)|((g&1)<<1))))<<4)+(pp&1)*8;
    f32x4 ak[8],av[8];
    #pragma unroll
    for(int t=0;t<8;t++){ ak[t]=f32x4{1,1,1,1}; av[t]=f32x4{0,0,0,0}; }
    bf16x8 xf[2][8], wf[2][2];
    auto rd1=[&](int s,int ks,int buf,int i){
      if(i<2){ ldscp base=(ldscp)(smem+(s&3)*ASLOT+wo+ks*4096+i*8192); wf[buf][i]=cat(ldstr(base),ldstr(base+512)); }
      else { const int t=i-2; const int o=(t<4)?(RES+s*8192+t*2048):(RING+(s&3)*ASLOT+(t-4)*2048);
             xf[buf][t]=lds128((ldscp)(smem+o+(xo^(ks*64)))); }
    };
    auto mm1=[&](int buf,int i){
      const int t=i>>1;
      if((i&1)==0) ak[t]=MFMA16(xf[buf][t],wf[buf][0],ak[t]); else av[t]=MFMA16(xf[buf][t],wf[buf][1],av[t]);
    };
    const unsigned fbase=WS_FIN+(p*32+cg*4+(rt&3))*FINH;
    const unsigned fslot=fbase+((rt>>2)&1)*256+4*n*16+(rt>>3)*8, zslot=fbase+512+((rt>>2)&1)*16+(rt>>3)*8;
    waitbar<12>();
        #pragma unroll
    for(int i=0;i<10;i++) rd1(0,0,0,i);
    SB();
    #pragma unroll
    for(int s=0;s<8;s++){
      #pragma unroll
      for(int i=0;i<16;i++){ mm1(0,i); if(i<10) rd1(s,1,1,i); SB(); }
      if(s==0) waitbar<8>(); else if(s==1) waitbar<8>(); else if(s==2) waitbar<4>(); else if(s==3) waitbar<0>(); else lbar();
      if(s==0){
        const u32x4 inv={0xffffffffu,0xffffffffu,0xffffffffu,0xffffffffu};
        #pragma unroll
        for(int u=0;u<4;u++) ST16(inv,rs,lane*16,WS_PQ+(((p*32+tt)*32+c*8+2*n)*2+u)*1024);
        const u32x2 inv2={0xffffffffu,0xffffffffu};
        if(lane<4) ST8(inv2,rs,fslot+lane*16,0);
        if(n==0 && lane==0) ST8(inv2,rs,zslot,0);
      }
      if(s==3 && wid==0 && lane==0) __builtin_amdgcn_raw_buffer_store_b32(tag,rs,WS_CLR+p*512+(rt*8+cg)*4,0,SC1);
      SB();
      #pragma unroll
      for(int i=0;i<16;i++){ mm1(1,i); if(s<7 && i<10) rd1(s+1,0,0,i); SB(); }
    }
    {
      f32x4 kv={0,0,0,0}, zz={0,0,0,0};
      const u32x4 ones={0x3f803f80u,0x3f803f80u,0x3f803f80u,0x3f803f80u};
      #pragma unroll
      for(int u=0;u<4;u++){
        u32x4 a={phi2(ak[2*u][0],ak[2*u][1]),phi2(ak[2*u][2],ak[2*u][3]),phi2(ak[2*u+1][0],ak[2*u+1][1]),phi2(ak[2*u+1][2],ak[2*u+1][3])};
        u32x4 v={pk2(av[2*u][0],av[2*u][1]),pk2(av[2*u][2],av[2*u][3]),pk2(av[2*u+1][0],av[2*u+1][1]),pk2(av[2*u+1][2],av[2*u+1][3])};
        kv=MFMA16(__builtin_bit_cast(bf16x8,a),__builtin_bit_cast(bf16x8,v),kv);
        zz=MFMA16(__builtin_bit_cast(bf16x8,ones),__builtin_bit_cast(bf16x8,a),zz);
      }
      const unsigned ph=p*32+h;
      const unsigned so=WS_PKV+(ph*16+rt)*2048;
      u32x4 g0={asu(kv[0]),tag,asu(kv[1]),tag}, g1={asu(kv[2]),tag,asu(kv[3]),tag};
      ST16(g0,rs,lane*16,so); ST16(g1,rs,lane*16,so+1024);
      if(lane<16){ u32x2 z0={asu(zz[0]),tag}; ST8(z0,rs,lane*8,WS_PZ+(ph*16+rt)*128); }
    }
    const unsigned vo=(lane>>2)*1024+(((lane&3)^(((lane>>5)&1)<<1))<<4);
    auto issueW=[&](int i){
      #pragma unroll
      for(int ii=0;ii<4;ii++) dma16(ro,lbase+wloc(i)+n*4096+ii*1024,vo,(((i*64)^(c<<7))+ii*16)*1024+c*256+n*64);
    };
    rbar();
    issueW(0); issueW(1); issueW(2);
    rbar();
        issueW(3); issueW(4); issueW(5);
    {
      const int hr=cg*4+(rt&3), qd=rt>>2;
      const unsigned ph=p*32+hr;
      const int prt=lane>>2, cc=lane&3;
      const unsigned o1=prt*2048+cc*16, s1o=WS_PKV+ph*16*2048+(qd*16+4*n)*16;
      const unsigned oz=prt*128+cc*8, szo=WS_PZ+ph*16*128+qd*32;
      u32x4 a,c2; u32x2 z={0,tag};
      #pragma nounroll
      for(int it=0;it<SPIN_MAX;it++){
        asm volatile("":::"memory");
        a=LD16(rs,o1,s1o); c2=LD16(rs,o1,s1o+1024);
        if(n==0) z=LD8(rs,oz,szo);
        const bool ok=(a[1]==tag)&(a[3]==tag)&(c2[1]==tag)&(c2[3]==tag)&(z[1]==tag);
        if(__all(ok)) break;
      }
      const float s0=sum_hi4(asf(a[0])), s1=sum_hi4(asf(a[2])), s2=sum_hi4(asf(c2[0])), s3=sum_hi4(asf(c2[2]));
      u32x2 f={pk2(s0,s1),pk2(s2,s3)};
      if(lane<4) ST8(f,rs,fslot+cc*16,0);
      if(n==0){
        const float z0=sum_hi4(asf(z[0]));
        const float z1=asf(__builtin_amdgcn_update_dpp(0,__builtin_bit_cast(int,z0),0x55,0xf,0xf,false));
        const float z2=asf(__builtin_amdgcn_update_dpp(0,__builtin_bit_cast(int,z0),0xaa,0xf,0xf,false));
        const float z3=asf(__builtin_amdgcn_update_dpp(0,__builtin_bit_cast(int,z0),0xff,0xf,0xf,false));
        u32x2 fz={pk2(z0,z1),pk2(z2,z3)};
        if(lane==0) ST8(fz,rs,zslot,0);
      }
    }
        rbar();
        const int ao0=n*4096+(4*hh+qq)*64+((((lane>>4)&1)*2+(pp>>1))<<4)+(pp&1)*8, ao1=(ao0+512)^32;
    bf16x8 af[32];
    auto rdaf=[&](int pos){ const int o=wloc(pos>>2)+(pos&3)*1024; af[pos]=cat(ldstr((ldscp)(smem+o+ao0)),ldstr((ldscp)(smem+o+ao1))); };
    #pragma unroll
    for(int pos=0;pos<24;pos++) rdaf(pos);
    SB();
    issueW(6); issueW(7);
    f32x16 acc[2];
    #pragma unroll
    for(int i=0;i<16;i++){ acc[0][i]=0.f; acc[1][i]=0.f; }
    bf16x8 bfr[8];
        lbar();
        #pragma unroll
    for(int i=0;i<8;i++) bfr[i]=lds128((ldscp)(smem+ostloc(0)+i*1024+lane*16));
    SB();
    #pragma unroll
    for(int hr=0;hr<8;hr++){
      const int rd=hr>>1;
      if((hr&1) && rd<3) rbar();
      if(hr==3) waitvm<0>();
      SB();
      #pragma unroll
      for(int i=0;i<8;i++){
        const int f=(hr&1)*8+i;
        acc[f&1]=MFMA32(af[rd*8+(f>>1)],bfr[i],acc[f&1]);
        if(hr<7){ const int nh=hr+1; bfr[i]=lds128((ldscp)(smem+ostloc(nh>>1)+((nh&1)*8+i)*1024+lane*16)); }
        if(hr==3) rdaf(24+i);
        SB();
      }
    }
        if(rt==0 && cg==0 && n==0 && lane==0) __builtin_amdgcn_raw_buffer_store_b32(tag,rs,WS_EPOCH+p*128,0,SC1);
    const rsrc_t rout=mkrsrc(out,4u<<20);
    #pragma unroll
    for(int tb=0;tb<2;tb++){
      const int row=tb*32+(lane&31);
      #pragma unroll
      for(int qd=0;qd<2;qd++){
        unsigned a0=pk2(acc[tb][8*qd+0],acc[tb][8*qd+1]), a1=pk2(acc[tb][8*qd+2],acc[tb][8*qd+3]);
        unsigned b0=pk2(acc[tb][8*qd+4],acc[tb][8*qd+5]), b1=pk2(acc[tb][8*qd+6],acc[tb][8*qd+7]);
        auto s0=__builtin_amdgcn_permlane32_swap(a0,b0,false,false);
        auto s1=__builtin_amdgcn_permlane32_swap(a1,b1,false,false);
        u32x4 v={s0[0],s1[0],s0[1],s1[1]};
        *(u32x4*)(smem+ostloc(0)+row*256+(((n*4+qd*2+hh)^(lane&15))<<4))=v;
      }
    }
    lbar();
    {
      u32x4 v[4];
      #pragma unroll
      for(int i=0;i<4;i++){ const int row=n*16+i*4+g; v[i]=__builtin_bit_cast(u32x4,lds128((ldscp)(smem+ostloc(0)+row*256+((l15^(row&15))<<4)))); }
      #pragma unroll
      for(int i=0;i<4;i++){ const int row=n*16+i*4+g; __builtin_amdgcn_raw_buffer_store_b128(v[i],rout,(unsigned)(tt*64+row)*2048+p*1024+c*256+l15*16,0,OUTAUX); }
    }
      }
}

extern "C" void kernel_launch(void* const* d_in, const int* in_sizes, int n_in, void* d_out, int out_size, void* d_ws, size_t ws_size, hipStream_t stream){
  k_fused<<<256,512,0,stream>>>((const u16*)d_in[0],(const u16*)d_in[1],(const u16*)d_in[2],(const u16*)d_in[3],(const u16*)d_in[4],(const u16*)d_in[5],(char*)d_ws,(u16*)d_out);
}
```

```cpp
#include <hip/hip_runtime.h>
typedef unsigned short u16;
typedef __attribute__((ext_vector_type(8))) __bf16 bf16x8;
typedef __attribute__((ext_vector_type(2))) __bf16 bf16x2;
typedef __attribute__((ext_vector_type(2))) float f32x2;
typedef __attribute__((ext_vector_type(4))) float f32x4;
typedef __attribute__((ext_vector_type(16))) float f32x16;
typedef __attribute__((ext_vector_type(4))) short s16x4;
typedef __attribute__((ext_vector_type(4))) unsigned u32x4;
typedef __attribute__((ext_vector_type(2))) unsigned u32x2;
typedef __attribute__((address_space(3))) void* ldsp;
typedef __attribute__((address_space(3))) const char* ldscp;
typedef __amdgpu_buffer_rsrc_t rsrc_t;
#define DI __device__ __forceinline__

DI unsigned pk2(float a,float b){ f32x2 v={a,b}; bf16x2 r=__builtin_convertvector(v,bf16x2); return __builtin_bit_cast(unsigned,r); }
DI float vmaxf(float a,float b){ return __builtin_amdgcn_fmed3f(a,b,3.402823466e38f); }
DI unsigned phi2(float t1a,float t1b){
  const float L=1.44269504088896340736f;
  f32x2 t={t1a,t1b};
  f32x2 a=t*f32x2{L,L}+f32x2{-L,-L};
  float e0=__builtin_amdgcn_fmed3f(__builtin_amdgcn_exp2f(a[0]),0.f,1.f), e1=__builtin_amdgcn_fmed3f(__builtin_amdgcn_exp2f(a[1]),0.f,1.f);
  f32x2 m={vmaxf(t1a,e0),vmaxf(t1b,e1)};
  m+=f32x2{1e-6f,1e-6f};
  bf16x2 r=__builtin_convertvector(m,bf16x2); return __builtin_bit_cast(unsigned,r);
}
DI bf16x8 lds128(ldscp p){ return *(const __attribute__((address_space(3))) bf16x8*)p; }
DI s16x4 ldstr(ldscp p){ return __builtin_amdgcn_ds_read_tr16_b64_v4i16((__attribute__((address_space(3))) s16x4*)p); }
DI bf16x8 cat(s16x4 lo,s16x4 hi){ return __builtin_bit_cast(bf16x8,__builtin_shufflevector(lo,hi,0,1,2,3,4,5,6,7)); }
DI ldscp lp(unsigned a){ return (ldscp)a; }
DI void dma16(rsrc_t rs,unsigned m0v,unsigned voff,unsigned soff){
  asm volatile("s_mov_b32 m0, %0\n\ts_nop 0\n\tbuffer_load_dwordx4 %1, %2, %3 offen lds"::"s"(m0v),"v"(voff),"s"(rs),"s"(soff):"memory");
}
DI void dma4(rsrc_t rs,unsigned m0v,unsigned voff,unsigned soff){
  asm volatile("s_mov_b32 m0, %0\n\ts_nop 0\n\tbuffer_load_dword %1, %2, %3 offen lds"::"s"(m0v),"v"(voff),"s"(rs),"s"(soff):"memory");
}
template<int N> DI void waitbar(){ asm volatile("s_waitcnt vmcnt(%0) lgkmcnt(0)\n\ts_barrier"::"n"(N):"memory"); }
template<int N> DI void waitvm(){ asm volatile("s_waitcnt vmcnt(%0)"::"n"(N):"memory"); }
DI void waitlg(){ asm volatile("s_waitcnt lgkmcnt(0)":::"memory"); }
DI void lbar(){ asm volatile("s_waitcnt lgkmcnt(0)\n\ts_barrier":::"memory"); }
DI void rbar(){ asm volatile("s_barrier":::"memory"); }
DI rsrc_t mkrsrc(const void* p,unsigned bytes){
  unsigned long long a=(unsigned long long)p;
  unsigned lo=__builtin_amdgcn_readfirstlane((unsigned)a), hi=__builtin_amdgcn_readfirstlane((unsigned)(a>>32));
  return __builtin_amdgcn_make_buffer_rsrc((void*)(((unsigned long long)hi<<32)|lo),0,bytes,0x00020000);
}
#define SB() __builtin_amdgcn_sched_barrier(0)
#define MFMA16(a,b,c) __builtin_amdgcn_mfma_f32_16x16x32_bf16((a),(b),(c),0,0,0)
#define MFMA32(a,b,c) __builtin_amdgcn_mfma_f32_32x32x16_bf16((a),(b),(c),0,0,0)
#define SC1 16
#define LD16(rs,vo,so) __builtin_amdgcn_raw_buffer_load_b128((rs),(vo),(so),SC1)
#define LD8(rs,vo,so) __builtin_amdgcn_raw_buffer_load_b64((rs),(vo),(so),SC1)
#define ST16(v,rs,vo,so) __builtin_amdgcn_raw_buffer_store_b128((v),(rs),(vo),(so),SC1)
#define ST8(v,rs,vo,so) __builtin_amdgcn_raw_buffer_store_b64((v),(rs),(vo),(so),SC1)

#define PF 1
#define ROTX ((r+2*c)&7)
#define PRIO_C 2
#define PRIO_H 0
#define HPRIO2 0
#define OUTAUX 16
#define KVROLL 1
#define PROLL 0
#define MRORD 1
#define SPINM 200000
constexpr int SPIN_MAX=SPINM;
constexpr unsigned WS_EPOCH=0;
constexpr unsigned WS_CLR=1024;
constexpr unsigned FINH=640;
constexpr unsigned WS_FIN=4096;
constexpr unsigned WS_PZ=WS_FIN+64*FINH;
constexpr unsigned WS_PKV=WS_PZ+64*16*128;
constexpr unsigned WS_PQ=WS_PKV+64*16*2048;
constexpr unsigned WS_END=WS_PQ+(4u<<20);
constexpr int SLOT=40960;

DI float asf(unsigned u){ return __builtin_bit_cast(float,u); }
DI unsigned asu(float f){ return __builtin_bit_cast(unsigned,f); }
DI float sum_hi4(float x){
  x+=asf(__builtin_amdgcn_update_dpp(0,__builtin_bit_cast(int,x),0x124,0xf,0xf,false));
  x+=asf(__builtin_amdgcn_update_dpp(0,__builtin_bit_cast(int,x),0x128,0xf,0xf,false));
  { auto s=__builtin_amdgcn_permlane16_swap(asu(x),asu(x),false,false); x=asf(s[0])+asf(s[1]); }
  { auto s=__builtin_amdgcn_permlane32_swap(asu(x),asu(x),false,false); x=asf(s[0])+asf(s[1]); }
  return x;
}
DI constexpr int wloc(int i){ return (i>>1)*SLOT+(i&1)*16384; }
DI constexpr int ostf(int r,int i){ return r<2 ? (2*r+(i>>3))*SLOT+32768+(i&7)*1024 : wloc(r-2)+i*1024; }
constexpr int OUTB=SLOT;

__global__ __launch_bounds__(512,2) void k_fused(const u16* __restrict__ x0,const u16* __restrict__ w0,const u16* __restrict__ wo0,
                                                 const u16* __restrict__ x1,const u16* __restrict__ w1,const u16* __restrict__ wo1,
                                                 char* __restrict__ ws,u16* __restrict__ out){
  __shared__ __attribute__((aligned(1024))) char smem[163840];
  const int wid=__builtin_amdgcn_readfirstlane(threadIdx.x>>6), lane=threadIdx.x&63;
  const unsigned lbase=(unsigned)(size_t)(ldsp)smem;
  const int b=blockIdx.x, xcd=b&7, idx=b>>3;
  const int p=xcd>>2, q=xcd&3, r=idx&7, c=idx>>3;
  const int half=q>>1, rt=(q&1)*8+r, cg=half*4+c, tt=2*rt+half;
  const rsrc_t rx=mkrsrc((p?x1:x0)+(size_t)rt*128*512,128*1024);
  const rsrc_t rw=mkrsrc(p?w1:w0,512*3072);
  const int rot=ROTX;
  const unsigned vx0=(lane>>3)*1024+(((lane&7)^(lane>>4))<<4), vx1=vx0^64;
  const unsigned vw0=(lane>>3)*3072+(((lane&7)^(((lane>>4)&1)<<2))<<4), vw1=vw0^32;
  const int j=wid&3;
  auto issueA=[&](int s){
    const int ka=(s+rot)&7;
    const unsigned lb=lbase+(s&3)*SLOT;
    #pragma unroll
    for(int ii=0;ii<4;ii++) dma16(rx,lb+(4*j+ii)*1024,(ii&1)?vx1:vx0,(4*j+ii)*8192+ka*128);
    #pragma unroll
    for(int ii=0;ii<6;ii++){ const int kb=2*j+(ii&1), blk=ii>>1;
      dma16(rw,lb+16384+blk*8192+kb*1024,(ii&1)?vw1:vw0,(ka*64+kb*8)*3072+blk*1024+cg*128); }
  };
  #pragma unroll
  for(int s=0;s<2;s++) issueA((wid>>2)*2+s);
  const rsrc_t ro=mkrsrc(p?wo1:wo0,512*1024);
  const rsrc_t rs=mkrsrc(ws,WS_END);
  unsigned ep;
  { const char* ea=ws+WS_EPOCH+p*128;
    asm volatile("s_load_dword %0, %1, 0x0\n\ts_waitcnt lgkmcnt(0)":"=s"(ep):"s"(ea):"memory"); }
  const unsigned tag=ep+1u;
  const int l15=lane&15, g=lane>>4;
  const int qq=l15>>2, pp=l15&3, hh=lane>>5;
  if(wid>=4){
    if(PRIO_H) __builtin_amdgcn_s_setprio(PRIO_H);
    const unsigned vo=(lane>>2)*1024+(((lane&3)^(((lane>>5)&1)<<1))<<4);
    auto issueW=[&](int i){
      #pragma unroll
      for(int ii=0;ii<4;ii++) dma16(ro,lbase+wloc(i)+j*4096+ii*1024,vo,(((i*64)^(c<<7))+ii*16)*1024+c*256+j*64);
    };
    const unsigned fbase=WS_FIN+(p*32+cg*4+(rt&3))*FINH;
    const unsigned fslot=fbase+(rt>>3)*256+4*j*16+((rt>>2)&1)*8, zslot=fbase+512+(rt>>3)*16+((rt>>2)&1)*8;
    rbar();
    rbar();           issueA(4);
    unsigned pfd=0;
    if(PF){
      const unsigned L=r*128+j*32+(lane>>1);
      asm volatile("buffer_load_dword %0, %1, %2, 0 offen":"=v"(pfd):"v"((L>>1)*1024+(L&1)*128+c*256+(lane&1)*64),"s"(ro):"memory");
    }
    {
      const u32x4 inv={0xffffffffu,0xffffffffu,0xffffffffu,0xffffffffu};
      const unsigned pqo=WS_PQ+((p*16+rt)*32+cg*4+j)*4096;
      #pragma unroll
      for(int u=0;u<4;u++) ST16(inv,rs,lane*16+u*1024,pqo);
      const u32x2 inv2={0xffffffffu,0xffffffffu};
      ST8(inv2,rs,lane<4?fslot+lane*16:((lane==4&&j==0)?zslot:WS_END),0);
    }
    waitbar<25+PF>(); issueA(5);
    waitbar<25+PF>(); issueA(6);
    waitbar<25+PF>(); issueA(7);
    waitbar<20>();    asm volatile(""::"v"(pfd));
    __builtin_amdgcn_raw_buffer_store_b32(tag,rs,(lane==0&&j==0)?WS_CLR+p*512+(rt*8+cg)*4:WS_END,0,SC1);
    issueW(0); issueW(1);
    waitbar<19>();    issueW(2); issueW(3);
    waitbar<17>();
    const u32x2 clr0=LD8(rs,lane*8,WS_CLR+p*512);
    issueW(4); issueW(5);
    rbar();           issueW(6); issueW(7);
    rbar();
    if(!__all((clr0[0]==tag)&(clr0[1]==tag))){
      #pragma nounroll
      for(int it=0;it<SPIN_MAX;it++){
        const u32x2 cl=LD8(rs,lane*8,WS_CLR+p*512);
        if(__all((cl[0]==tag)&(cl[1]==tag))) break;
        asm volatile("":::"memory");
      }
    }
    const unsigned fo=(lane&16)?(512+hh*16):(hh*256+l15*16);
    unsigned fso[4], pso[4];
    #pragma unroll
    for(int rd=0;rd<4;rd++){ fso[rd]=WS_FIN+(p*32+((rd^c)*8)+2*j)*FINH; pso[rd]=WS_PQ+((p*16+rt)*32+(rd^c)*8+2*j)*4096+half*2048; asm volatile("":"+s"(fso[rd]),"+s"(pso[rd])); }
    u32x4 pq[4][2][2];
    auto pload=[&](int rd,int i){ return LD16(rs,lane*16+(i>>1)*4096+(i&1)*1024,pso[rd]); };
    #pragma unroll
    for(int rd=0;rd<4;rd++){
      #pragma unroll
      for(int i=0;i<4;i++) pq[rd][i>>1][i&1]=pload(rd,i);
    }
    auto pvalid=[&](int rd){ unsigned m=0;
      #pragma unroll
      for(int i=0;i<4;i++){ const u32x4 v=pq[rd][i>>1][i&1]; m|=(v[0]|v[1])|(v[2]|v[3]); }
      return (bool)__all((m&0x80008000u)==0); };
    #pragma unroll
    for(int rd=0;rd<4;rd++){
      if(!pvalid(rd)){
        #pragma nounroll
        for(int it=0;it<SPIN_MAX;it++){
          asm volatile("":::"memory");
          #pragma unroll
          for(int i=0;i<4;i++) pq[rd][i>>1][i&1]=pload(rd,i);
          if(pvalid(rd)) break;
        }
      }
    }
    waitbar<0>();
    u32x4 fin[4][2];
    #pragma nounroll
    for(int it=0;it<SPIN_MAX;it++){
      asm volatile("":::"memory");
      unsigned m=0;
      #pragma unroll
      for(int rd=0;rd<4;rd++){
        #pragma unroll
        for(int hs=0;hs<2;hs++) fin[rd][hs]=LD16(rs,fo+hs*FINH,fso[rd]);
      }
      bool ok=true;
      #pragma unroll
      for(int rd=0;rd<4;rd++){
        #pragma unroll
        for(int hs=0;hs<2;hs++){ const u32x4 v=fin[rd][hs]; m=max(max(v[0],v[1]),max(v[2],v[3])); ok&=(m!=0xffffffffu); }
      }
      if(__all(ok)) break;
    }
    if(HPRIO2) __builtin_amdgcn_s_setprio(HPRIO2);
    const f32x16 zero16={0,0,0,0,0,0,0,0,0,0,0,0,0,0,0,0};
    const unsigned oa_lo=lbase+lane*16+(j>>1)*SLOT+(j&1)*4096, oa_hi=lbase+lane*16+j*4096;
    #pragma unroll
    for(int rd=0;rd<4;rd++){
      f32x16 d[2][2];
      #pragma unroll
      for(int hs=0;hs<2;hs++){
        d[hs][0]=MFMA32(__builtin_bit_cast(bf16x8,fin[rd][hs]),__builtin_bit_cast(bf16x8,pq[rd][hs][0]),zero16);
        d[hs][1]=MFMA32(__builtin_bit_cast(bf16x8,fin[rd][hs]),__builtin_bit_cast(bf16x8,pq[rd][hs][1]),zero16);
      }
      SB();
      #pragma unroll
      for(int hs=0;hs<2;hs++){
        #pragma unroll
        for(int u=0;u<2;u++){
          const f32x16 dd=d[hs][u];
          float rc=__builtin_amdgcn_rcpf(dd[8]+1e-6f);
          u32x4 o={pk2(dd[0]*rc,dd[1]*rc),pk2(dd[2]*rc,dd[3]*rc),pk2(dd[4]*rc,dd[5]*rc),pk2(dd[6]*rc,dd[7]*rc)};
          const unsigned oa=(rd<2?oa_lo+rd*2*SLOT+32768:oa_hi+wloc(rd-2))+(hs*2+u)*1024;
          *(__attribute__((address_space(3))) u32x4*)oa=o;
        }
      }
      SB();
      lbar();
      SB();
    }
  } else {
    const int n=wid;
    if(PRIO_C) __builtin_amdgcn_s_setprio(PRIO_C);
    const int h=cg*4+n;
    f32x4 ak[8],av[8],aq[8];
    #pragma unroll
    for(int t=0;t<8;t++){ ak[t]=f32x4{1,1,1,1}; av[t]=f32x4{0,0,0,0}; aq[t]=f32x4{1,1,1,1}; }
    bf16x8 xf[2][8], wf[2][3];
    unsigned xa0=lbase+l15*128+((g^(l15>>1))<<4), xa1=xa0^64;
    unsigned wa=lbase+16384+(8*g+qq)*128+((((2*n+(pp>>1))^((((qq>>1)&1)<<2)|((g&1)<<1))))<<4)+(pp&1)*8;
    auto rd1=[&](int ks,int i){
      if(i<3){ const int bl=(i==0)?1:(i==1?2:0); wf[ks][bl]=cat(ldstr(lp(wa+bl*8192+ks*4096)),ldstr(lp(wa+bl*8192+ks*4096+512))); }
      else xf[ks][i-3]=lds128(lp((ks?xa1:xa0)+(i-3)*2048));
    };
    auto mm1=[&](int buf,int i){
      const int t=i/3, w=i%3;
      if(w==0) ak[t]=MFMA16(xf[buf][t],wf[buf][1],ak[t]);
      else if(w==1) av[t]=MFMA16(xf[buf][t],wf[buf][2],av[t]);
      else aq[t]=MFMA16(wf[buf][0],xf[buf][t],aq[t]);
    };
    const unsigned fbase=WS_FIN+(p*32+cg*4+(rt&3))*FINH;
    const unsigned fslot=fbase+(rt>>3)*256+4*n*16+((rt>>2)&1)*8, zslot=fbase+512+(rt>>3)*16+((rt>>2)&1)*8;
    const unsigned pqo=WS_PQ+((p*16+rt)*32+h)*4096;
    waitbar<10>();
    #pragma unroll
    for(int i=0;i<11;i++) rd1(0,i);
    SB();
    #pragma unroll 1
    for(int s=0;s<8;s++){
      #pragma unroll
      for(int i=0;i<24;i++){ mm1(0,i); if(MRORD) SB(); if(i<11) rd1(1,i); SB(); }
      waitbar<0>();
      { const int d=((s&3)==3)?-3*SLOT:SLOT; xa0+=d; xa1+=d; wa+=d; }
      SB();
      #pragma unroll
      for(int i=0;i<24;i++){ mm1(1,i); if(MRORD) SB(); if(i<11 && (KVROLL || s<7)) rd1(0,i); SB(); }
    }
    {
      f32x4 kv={0,0,0,0}, zz={0,0,0,0};
      const u32x4 ones={0x3f803f80u,0x3f803f80u,0x3f803f80u,0x3f803f80u};
      #pragma unroll
      for(int u=0;u<4;u++){
        u32x4 a={phi2(ak[2*u][0],ak[2*u][1]),phi2(ak[2*u][2],ak[2*u][3]),phi2(ak[2*u+1][0],ak[2*u+1][1]),phi2(ak[2*u+1][2],ak[2*u+1][3])};
        u32x4 v={pk2(av[2*u][0],av[2*u][1]),pk2(av[2*u][2],av[2*u][3]),pk2(av[2*u+1][0],av[2*u+1][1]),pk2(av[2*u+1][2],av[2*u+1][3])};
        kv=MFMA16(__builtin_bit_cast(bf16x8,a),__builtin_bit_cast(bf16x8,v),kv);
        zz=MFMA16(__builtin_bit_cast(bf16x8,ones),__builtin_bit_cast(bf16x8,a),zz);
      }
      const unsigned ph=p*32+h;
      const unsigned so=WS_PKV+(ph*16+rt)*2048;
      u32x4 g0={asu(kv[0]),tag,asu(kv[1]),tag}, g1={asu(kv[2]),tag,asu(kv[3]),tag};
      ST16(g0,rs,lane*16,so); ST16(g1,rs,lane*16,so+1024);
      if(lane<16){ u32x2 z0={asu(zz[0]),tag}; ST8(z0,rs,lane*8,WS_PZ+(ph*16+rt)*128); }
    }
    SB();
    #pragma unroll
    for(int f=0;f<4;f++){
      const unsigned a0=phi2(aq[2*f][0],aq[2*f][1]), a1=phi2(aq[2*f][2],aq[2*f][3]);
      const unsigned b0=phi2(aq[2*f+1][0],aq[2*f+1][1]), b1=phi2(aq[2*f+1][2],aq[2*f+1][3]);
      auto s0=__builtin_amdgcn_permlane16_swap(a0,b0,false,false);
      auto s1=__builtin_amdgcn_permlane16_swap(a1,b1,false,false);
      u32x4 v={s0[0],s1[0],s0[1],s1[1]};
      ST16(v,rs,lane*16+f*1024,pqo);
    }
    SB();
    {
      const int hr=cg*4+(rt&3), qd=rt>>2;
      const unsigned ph=p*32+hr;
      const int prt=lane>>2, cc=lane&3;
      const unsigned o1=prt*2048+cc*16, s1o=WS_PKV+ph*16*2048+(qd*16+4*n)*16;
      const unsigned oz=prt*128+cc*8, szo=WS_PZ+ph*16*128+qd*32;
      u32x4 a,c2; u32x2 z={0,tag};
      a=LD16(rs,o1,s1o); c2=LD16(rs,o1,s1o+1024);
      if(n==0) z=LD8(rs,oz,szo);
      SB();
      rbar();
      SB();
      if(!__all((a[1]==tag)&(a[3]==tag)&(c2[1]==tag)&(c2[3]==tag)&(z[1]==tag))){
        #pragma nounroll
        for(int it=0;it<SPIN_MAX;it++){
          asm volatile("":::"memory");
          a=LD16(rs,o1,s1o); c2=LD16(rs,o1,s1o+1024);
          if(n==0) z=LD8(rs,oz,szo);
          const bool ok=(a[1]==tag)&(a[3]==tag)&(c2[1]==tag)&(c2[3]==tag)&(z[1]==tag);
          if(__all(ok)) break;
        }
      }
      const float s0=sum_hi4(asf(a[0])), s1=sum_hi4(asf(a[2])), s2=sum_hi4(asf(c2[0])), s3=sum_hi4(asf(c2[2]));
      u32x2 f={pk2(s0,s1),pk2(s2,s3)};
      if(lane<4) ST8(f,rs,fslot+cc*16,0);
      if(n==0){
        const float z0=sum_hi4(asf(z[0]));
        const float z1=asf(__builtin_amdgcn_update_dpp(0,__builtin_bit_cast(int,z0),0x55,0xf,0xf,false));
        const float z2=asf(__builtin_amdgcn_update_dpp(0,__builtin_bit_cast(int,z0),0xaa,0xf,0xf,false));
        const float z3=asf(__builtin_amdgcn_update_dpp(0,__builtin_bit_cast(int,z0),0xff,0xf,0xf,false));
        u32x2 fz={pk2(z0,z1),pk2(z2,z3)};
        if(lane==0) ST8(fz,rs,zslot,0);
      }
    }
    rbar();
    const int ao0=n*4096+(4*hh+qq)*64+((((lane>>4)&1)*2+(pp>>1))<<4)+(pp&1)*8, ao1=(ao0+512)^32;
    bf16x8 af[32];
    #pragma unroll
    for(int pos=0;pos<32;pos++){ const int o=wloc(pos>>2)+(pos&3)*1024; af[pos]=cat(ldstr((ldscp)(smem+o+ao0)),ldstr((ldscp)(smem+o+ao1))); }
    SB();
    f32x16 acc[2];
    #pragma unroll
    for(int i=0;i<16;i++){ acc[0][i]=0.f; acc[1][i]=0.f; }
    bf16x8 bfr[8];
    lbar();
    #pragma unroll
    for(int i=0;i<8;i++) bfr[i]=lds128((ldscp)(smem+ostf(0,i)+lane*16));
    SB();
    #pragma unroll
    for(int hr=0;hr<8;hr++){
      const int rd=hr>>1;
      if((hr&1) && rd<3) rbar();
      SB();
      #pragma unroll
      for(int i=0;i<8;i++){
        const int f=(hr&1)*8+i;
        acc[f&1]=MFMA32(af[rd*8+(f>>1)],bfr[i],acc[f&1]);
        if(hr<7){ const int nh=hr+1; bfr[i]=lds128((ldscp)(smem+ostf(nh>>1,(nh&1)*8+i)+lane*16)); }
        SB();
      }
    }
    if(rt==0 && cg==0 && n==0 && lane==0) __builtin_amdgcn_raw_buffer_store_b32(tag,rs,WS_EPOCH+p*128,0,SC1);
    const rsrc_t rout=mkrsrc(out,4u<<20);
    #pragma unroll
    for(int tb=0;tb<2;tb++){
      const int row=tb*32+(lane&31);
      #pragma unroll
      for(int qd=0;qd<2;qd++){
        unsigned a0=pk2(acc[tb][8*qd+0],acc[tb][8*qd+1]), a1=pk2(acc[tb][8*qd+2],acc[tb][8*qd+3]);
        unsigned b0=pk2(acc[tb][8*qd+4],acc[tb][8*qd+5]), b1=pk2(acc[tb][8*qd+6],acc[tb][8*qd+7]);
        auto s0=__builtin_amdgcn_permlane32_swap(a0,b0,false,false);
        auto s1=__builtin_amdgcn_permlane32_swap(a1,b1,false,false);
        u32x4 v={s0[0],s1[0],s0[1],s1[1]};
        *(u32x4*)(smem+OUTB+row*256+(((n*4+qd*2+hh)^(lane&15))<<4))=v;
      }
    }
    lbar();
    {
      u32x4 v[4];
      #pragma unroll
      for(int i=0;i<4;i++){ const int row=n*16+i*4+g; v[i]=__builtin_bit_cast(u32x4,lds128((ldscp)(smem+OUTB+row*256+((l15^(row&15))<<4)))); }
      #pragma unroll
      for(int i=0;i<4;i++){ const int row=n*16+i*4+g; __builtin_amdgcn_raw_buffer_store_b128(v[i],rout,(unsigned)(tt*64+row)*2048+p*1024+c*256+l15*16,0,OUTAUX); }
    }
  }
}

extern "C" void kernel_launch(void* const* d_in, const int* in_sizes, int n_in, void* d_out, int out_size, void* d_ws, size_t ws_size, hipStream_t stream){
  k_fused<<<256,512,0,stream>>>((const u16*)d_in[0],(const u16*)d_in[1],(const u16*)d_in[2],(const u16*)d_in[3],(const u16*)d_in[4],(const u16*)d_in[5],(char*)d_ws,(u16*)d_out);
}
```

```cpp
#include <hip/hip_runtime.h>
typedef unsigned short u16;
typedef __attribute__((ext_vector_type(8))) __bf16 bf16x8;
typedef __attribute__((ext_vector_type(2))) __bf16 bf16x2;
typedef __attribute__((ext_vector_type(2))) float f32x2;
typedef __attribute__((ext_vector_type(4))) float f32x4;
typedef __attribute__((ext_vector_type(16))) float f32x16;
typedef __attribute__((ext_vector_type(4))) short s16x4;
typedef __attribute__((ext_vector_type(4))) unsigned u32x4;
typedef __attribute__((ext_vector_type(2))) unsigned u32x2;
typedef __attribute__((address_space(3))) void* ldsp;
typedef __attribute__((address_space(3))) const char* ldscp;
typedef __amdgpu_buffer_rsrc_t rsrc_t;
#define DI __device__ __forceinline__

DI unsigned pk2(float a,float b){ f32x2 v={a,b}; bf16x2 r=__builtin_convertvector(v,bf16x2); return __builtin_bit_cast(unsigned,r); }
DI float mul1(float a,float b){ float r=a*b; asm(""  :"+v"(r)); return r; }
DI unsigned phi2(float t1a,float t1b){
  const float L=1.44269504088896340736f;
  f32x2 t={t1a,t1b};
  f32x2 a=t*f32x2{L,L}+f32x2{-L,-L};
  f32x2 m={__builtin_amdgcn_fmed3f(t1a,__builtin_amdgcn_exp2f(a[0]),1.f),__builtin_amdgcn_fmed3f(t1b,__builtin_amdgcn_exp2f(a[1]),1.f)};
  m+=f32x2{1e-6f,1e-6f};
  bf16x2 r=__builtin_convertvector(m,bf16x2); return __builtin_bit_cast(unsigned,r);
}
DI bf16x8 lds128(ldscp p){ return *(const __attribute__((address_space(3))) bf16x8*)p; }
DI s16x4 ldstr(ldscp p){ return __builtin_amdgcn_ds_read_tr16_b64_v4i16((__attribute__((address_space(3))) s16x4*)p); }
DI bf16x8 cat(s16x4 lo,s16x4 hi){ return __builtin_bit_cast(bf16x8,__builtin_shufflevector(lo,hi,0,1,2,3,4,5,6,7)); }
DI ldscp lp(unsigned a){ return (ldscp)a; }
DI void dma16(rsrc_t rs,unsigned m0v,unsigned voff,unsigned soff){
  asm volatile("s_mov_b32 m0, %0\n\ts_nop 0\n\tbuffer_load_dwordx4 %1, %2, %3 offen lds"::"s"(m0v),"v"(voff),"s"(rs),"s"(soff):"memory");
}
DI void dma4(rsrc_t rs,unsigned m0v,unsigned voff,unsigned soff){
  asm volatile("s_mov_b32 m0, %0\n\ts_nop 0\n\tbuffer_load_dword %1, %2, %3 offen lds"::"s"(m0v),"v"(voff),"s"(rs),"s"(soff):"memory");
}
template<int N> DI void waitbar(){ asm volatile("s_waitcnt vmcnt(%0) lgkmcnt(0)\n\ts_barrier"::"n"(N):"memory"); }
template<int N> DI void waitvm(){ asm volatile("s_waitcnt vmcnt(%0)"::"n"(N):"memory"); }
DI void waitlg(){ asm volatile("s_waitcnt lgkmcnt(0)":::"memory"); }
DI void lbar(){ asm volatile("s_waitcnt lgkmcnt(0)\n\ts_barrier":::"memory"); }
DI void rbar(){ asm volatile("s_barrier":::"memory"); }
DI rsrc_t mkrsrc(const void* p,unsigned bytes){
  unsigned long long a=(unsigned long long)p;
  unsigned lo=__builtin_amdgcn_readfirstlane((unsigned)a), hi=__builtin_amdgcn_readfirstlane((unsigned)(a>>32));
  return __builtin_amdgcn_make_buffer_rsrc((void*)(((unsigned long long)hi<<32)|lo),0,bytes,0x00020000);
}
#define SB() __builtin_amdgcn_sched_barrier(0)
#define MFMA16(a,b,c) __builtin_amdgcn_mfma_f32_16x16x32_bf16((a),(b),(c),0,0,0)
#define MFMA32(a,b,c) __builtin_amdgcn_mfma_f32_32x32x16_bf16((a),(b),(c),0,0,0)
#define SC1 16
#define LD16(rs,vo,so) __builtin_amdgcn_raw_buffer_load_b128((rs),(vo),(so),SC1)
#define LD8(rs,vo,so) __builtin_amdgcn_raw_buffer_load_b64((rs),(vo),(so),SC1)
#define ST16(v,rs,vo,so) __builtin_amdgcn_raw_buffer_store_b128((v),(rs),(vo),(so),SC1)
#define ST8(v,rs,vo,so) __builtin_amdgcn_raw_buffer_store_b64((v),(rs),(vo),(so),SC1)

constexpr int SPIN_MAX=200000;
constexpr int WS1=2, WP=2;
constexpr int OUTAUX=16;
constexpr unsigned WS_EPOCH=0;
constexpr unsigned WS_CLR=1024;
constexpr unsigned WS_PZ=4096;
constexpr unsigned WS_PKV=WS_PZ+64*16*64;
constexpr unsigned WS_O=WS_PKV+64*16*512;
constexpr unsigned WS_END=WS_O+(4u<<20);
constexpr unsigned TX=0x95186420u, TW=0x37397531u;
DI constexpr unsigned UX(int s){ return ((TX>>(4*s))&15u)*16384u; }
DI constexpr unsigned UW(int s){ return ((TW>>(4*s))&15u)*16384u; }
DI constexpr unsigned UQ(int s){ return ((0x00773377u>>(4*s))&15u)*16384u+(s&1)*8192u; }
DI constexpr unsigned UO(int i){ return ((0x95183642u>>(4*i))&15u)*16384u; }
constexpr unsigned FINB=2*16384;
DI constexpr int ostf(int r,int i){ return (r==0?0:r==1?7:r==2?2:4)*16384+i*1024; }
constexpr int OUTB=6*16384;
constexpr unsigned INV=0xffffffffu;

DI float asf(unsigned u){ return __builtin_bit_cast(float,u); }
DI unsigned asu(float f){ return __builtin_bit_cast(unsigned,f); }
DI unsigned san(unsigned u){ return u<0xfffffffeu?u:0xfffffffeu; }
DI float sum_hi4(float x){
  x+=asf(__builtin_amdgcn_update_dpp(0,__builtin_bit_cast(int,x),0x124,0xf,0xf,false));
  x+=asf(__builtin_amdgcn_update_dpp(0,__builtin_bit_cast(int,x),0x128,0xf,0xf,false));
  { auto s=__builtin_amdgcn_permlane16_swap(asu(x),asu(x),false,false); x=asf(s[0])+asf(s[1]); }
  { auto s=__builtin_amdgcn_permlane32_swap(asu(x),asu(x),false,false); x=asf(s[0])+asf(s[1]); }
  return x;
}
DI unsigned umax4(u32x4 v){ return max(max(v[0],v[1]),max(v[2],v[3])); }
DI void ldsw128(unsigned a,u32x4 v){ *(__attribute__((address_space(3))) u32x4*)(ldsp)a=v; }
DI void ldsw64(unsigned a,u32x2 v){ *(__attribute__((address_space(3))) u32x2*)(ldsp)a=v; }

__global__ __launch_bounds__(512,2) void k_fused(const u16* __restrict__ x0,const u16* __restrict__ w0,const u16* __restrict__ wo0,
                                                 const u16* __restrict__ x1,const u16* __restrict__ w1,const u16* __restrict__ wo1,
                                                 char* __restrict__ ws,u16* __restrict__ out){
  __shared__ __attribute__((aligned(1024))) char smem[163840];
  const int wid=__builtin_amdgcn_readfirstlane(threadIdx.x>>6), lane=threadIdx.x&63;
  const unsigned lbase=(unsigned)(size_t)(ldsp)smem;
  const int b=blockIdx.x, xcd=b&7, idx=b>>3;
  const int p=xcd>>2, q=xcd&3, r=idx&7, c=idx>>3;
  const int half=q>>1, rt=(q&1)*8+r, cg=half*4+c, tt=2*rt+half;
  const rsrc_t rx=mkrsrc((p?x1:x0)+(size_t)rt*128*512,128*1024);
  const rsrc_t rw=mkrsrc(p?w1:w0,512*3072);
  const int rot=(r+2*c)&7;
  const unsigned vx0=(lane>>3)*1024+(((lane&7)^(lane>>4))<<4), vx1=vx0^64;
  const unsigned vw0=(lane>>3)*3072+(((lane&7)^(((lane>>4)&1)<<2))<<4), vw1=vw0^32;
  const int j=wid&3;
  auto issueX=[&](int s){
    const int ka=(s+rot)&7;
    const unsigned lb=lbase+UX(s);
    #pragma unroll
    for(int ii=0;ii<4;ii++) dma16(rx,lb+(4*j+ii)*1024,(ii&1)?vx1:vx0,(4*j+ii)*8192+ka*128);
  };
  auto issueW=[&](int s){
    const int ka=(s+rot)&7;
    const unsigned lb=lbase+UW(s);
    #pragma unroll
    for(int ii=0;ii<4;ii++){ const int kb=2*j+(ii&1), blk=ii>>1;
      dma16(rw,lb+blk*8192+kb*1024,(ii&1)?vw1:vw0,(ka*64+kb*8)*3072+(blk+1)*1024+cg*128); }
  };
  const rsrc_t ro=mkrsrc(p?wo1:wo0,512*1024);
  const unsigned vo=(lane>>2)*1024+(((lane&3)^(((lane>>5)&1)<<1))<<4);
  unsigned pfd=0, pfd2=0, pfd3=0;
  const rsrc_t rout=mkrsrc(out,4u<<20);
  const rsrc_t rs=mkrsrc(ws,WS_END);
  #pragma unroll
  for(int s=0;s<2;s++){ issueX((wid>>2)*2+s); issueW((wid>>2)*2+s); }
  if(wid>=4){
    const unsigned L=r*128+j*32+(lane>>1);
    asm volatile("buffer_load_dword %0, %1, %2, 0 offen":"=v"(pfd):"v"((L>>1)*1024+(L&1)*128+c*256+(lane&1)*64),"s"(ro):"memory");
    asm volatile("buffer_load_dword %0, %1, %2, 0 offen":"=v"(pfd2):"v"((unsigned)(r*64+j*16+(lane>>2))*3072+cg*128+(lane&3)*32),"s"(rw):"memory");
    asm volatile("buffer_load_dword %0, %1, %2, 0 offen":"=v"(pfd3):"v"((unsigned)(tt*64+j*16)*2048+p*1024+c*256),"s"(rout):"memory");
  }
  unsigned ep;
  { const char* ea=ws+WS_EPOCH+p*128;
    asm volatile("s_load_dword %0, %1, 0x0\n\ts_waitcnt lgkmcnt(0)":"=s"(ep):"s"(ea):"memory"); }
  const unsigned tag=ep+1u;
  const int l15=lane&15, g=lane>>4;
  const int qq=l15>>2, pp=l15&3, hh=lane>>5;
  const unsigned xp0=lbase+l15*128+((g^(l15>>1))<<4);
  if(wid>=4){
    const unsigned ph=p*32+cg*4+j;
    const unsigned oso=WS_O+((p*16+rt)*32+cg*4+j)*4096;
    rbar();           issueX(4); issueW(4);
    rbar();           issueX(5);
    {
      const u32x4 inv={INV,INV,INV,INV};
      #pragma unroll
      for(int u=0;u<4;u++) ST16(inv,rs,lane*16+u*1024,oso);
      ST16(inv,rs,lane<32?WS_PKV+(ph*16+rt)*512+lane*16:WS_END,0);
      ST16(inv,rs,lane<4?WS_PZ+(ph*16+rt)*64+lane*16:WS_END,0);
    }
    waitbar<29>();    issueW(5);
    waitbar<25>(); issueX(6);
    waitbar<18>(); asm volatile(""::"v"(pfd),"v"(pfd2),"v"(pfd3));
    issueW(6);
    u32x4 w7[4];
    { const int ka=(7+rot)&7;
      #pragma unroll
      for(int ii=0;ii<4;ii++){ const int kb=2*j+(ii&1), blk=ii>>1;
        asm volatile("buffer_load_dwordx4 %0, %1, %2, %3 offen":"=v"(w7[ii]):"v"((ii&1)?vw1:vw0),"s"(rw),"s"((ka*64+kb*8)*3072+(blk+1)*1024+cg*128):"memory"); } }
    waitbar<12>(); issueX(7);
    u32x4 wr[8][2];
    auto wqload=[&](int s){ const int ka=(s+rot)&7;
      #pragma unroll
      for(int i=0;i<2;i++) wr[s][i]=__builtin_amdgcn_raw_buffer_load_b128(rw,i?vw1:vw0,(ka*64+(2*j+i)*8)*3072+cg*128,0); };
    #pragma unroll
    for(int s=0;s<4;s++) wqload(s);
    __builtin_amdgcn_raw_buffer_store_b32(tag,rs,(lane==0&&j==0)?WS_CLR+p*512+(rt*8+cg)*4:WS_END,0,SC1);
    waitbar<13>();
    #pragma unroll
    for(int ii=0;ii<4;ii++) asm volatile("ds_write_b128 %0, %1"::"v"(lbase+UW(7)+(ii>>1)*8192+(2*j+(ii&1))*1024+lane*16),"v"(w7[ii]):"memory");
    #pragma unroll
    for(int s=4;s<8;s++) wqload(s);
    waitbar<13>();
    unsigned wwl=lbase+j*2048+lane*16, wwh=wwl+65536;
    asm volatile("":"+v"(wwl),"+v"(wwh));
    auto wqwrite=[&](int s){ const unsigned a=(UQ(s)>>16?wwh:wwl)+(UQ(s)&65535); ldsw128(a,wr[s][0]); ldsw128(a+1024,wr[s][1]); };
    wqwrite(0); wqwrite(1);
    SB();
    SB();
    lbar();
    SB();
    wqwrite(2); wqwrite(3);
    const int rh=j&1, hp=j>>1;
    f32x4 aq[2][4];
    #pragma unroll
    for(int t=0;t<4;t++){ aq[0][t]=f32x4{1,1,1,1}; aq[1][t]=f32x4{1,1,1,1}; }
    bf16x8 xf[2][4], wf[2][2];
    const unsigned xq=xp0+rh*8192;
    unsigned xb[3][2]={{xq,xq^64},{xq+65536,(xq^64)+65536},{xq+131072,(xq^64)+131072}};
    unsigned wqa[2][2];
    { const unsigned w0a=lbase+(8*g+qq)*128+((((pp>>1))^((((qq>>1)&1)<<2)|((g&1)<<1)))<<4)+(pp&1)*8;
      #pragma unroll
      for(int i=0;i<2;i++){ wqa[0][i]=w0a^((2*hp+i)<<5); wqa[1][i]=wqa[0][i]+65536; asm volatile("":"+v"(wqa[0][i]),"+v"(wqa[1][i])); } }
    #pragma unroll
    for(int i=0;i<3;i++) asm volatile("":"+v"(xb[i][0]),"+v"(xb[i][1]));
    auto qrd=[&](int hs,int i){
      const int s=hs>>1, ks=hs&1;
      if(i==1||i==2){ const unsigned a=wqa[UQ(s)>>16][i-1]+(UQ(s)&65535)+ks*4096; wf[ks][i-1]=cat(ldstr(lp(a)),ldstr(lp(a+512))); }
      else { const int u=i?i-2:0; xf[ks][u]=lds128(lp(xb[UX(s)>>16][ks]+(UX(s)&65535)+u*2048)); }
    };
    #pragma unroll
    for(int i=0;i<6;i++) qrd(0,i);
    SB();
    #pragma unroll
    for(int hs=0;hs<16;hs++){
      #pragma unroll
      for(int t=0;t<8;t++){
        aq[t&1][t>>1]=MFMA16(wf[hs&1][t&1],xf[hs&1][t>>1],aq[t&1][t>>1]); SB();
        if(hs<15 && t<6) qrd(hs+1,t);
        if(hs==3 && t>=6){ wqwrite(4+(t-6)); wqwrite(6+(t-6)); }
        SB();
      }
      if(hs==2){ lbar(); SB(); }
      if(hs==6){ lbar(); SB(); }
      if(hs==14){ SB(); }
    }
    u32x4 pq[4];
    #pragma unroll
    for(int f=0;f<4;f++){
      const f32x4 qa=aq[f>>1][2*(f&1)], qb=aq[f>>1][2*(f&1)+1];
      const unsigned a0=phi2(qa[0],qa[1]), a1=phi2(qa[2],qa[3]);
      const unsigned b0=phi2(qb[0],qb[1]), b1=phi2(qb[2],qb[3]);
      auto s0=__builtin_amdgcn_permlane16_swap(a0,b0,false,false);
      auto s1=__builtin_amdgcn_permlane16_swap(a1,b1,false,false);
      pq[f]=u32x4{s0[0],s1[0],s0[1],s1[1]};
    }
    SB();
    rbar();
    SB();
    {
      const unsigned fa=lbase+FINB+2*hp*4096+((lane&16)?(512+hh*16):(hh*256+l15*16));
      bf16x8 fin[2];
      #pragma unroll
      for(int f=0;f<2;f++) fin[f]=lds128(lp(fa+f*4096));
      const f32x16 zero16={0,0,0,0,0,0,0,0,0,0,0,0,0,0,0,0};
      f32x16 d[4];
      #pragma unroll
      for(int f=0;f<4;f++) d[f]=MFMA32(fin[f>>1],__builtin_bit_cast(bf16x8,pq[f]),zero16);
      const unsigned ow=WS_O+((p*16+rt)*32+cg*4+2*hp)*4096+2*rh*1024+lane*16;
      #pragma unroll
      for(int f=0;f<4;f++){
        const f32x16 dd=d[f];
        const float rc=__builtin_amdgcn_rcpf(dd[8]+1e-6f);
        u32x4 o={san(pk2(mul1(dd[0],rc),mul1(dd[1],rc))),san(pk2(mul1(dd[2],rc),mul1(dd[3],rc))),san(pk2(mul1(dd[4],rc),mul1(dd[5],rc))),san(pk2(mul1(dd[6],rc),mul1(dd[7],rc)))};
        ST16(o,rs,ow+(f>>1)*4096+(f&1)*1024,0);
      }
    }
    SB();
    rbar(); SB();
    unsigned pso[4];
    #pragma unroll
    for(int rd=0;rd<4;rd++){ pso[rd]=WS_O+((p*16+rt)*32+(rd^c)*8+2*j)*4096+half*2048; asm volatile("":"+s"(pso[rd])); }
    u32x4 og[4][4];
    auto oload=[&](int rd,int i){ return LD16(rs,lane*16+(i>>1)*4096+(i&1)*1024,pso[rd]); };
    #pragma unroll
    for(int rd=0;rd<4;rd++){
      #pragma unroll
      for(int i=0;i<4;i++) og[rd][i]=oload(rd,i);
      SB();
    }
    auto ovalid=[&](int rd){ return (bool)__all(max(max(umax4(og[rd][0]),umax4(og[rd][1])),max(umax4(og[rd][2]),umax4(og[rd][3])))!=INV); };
    #pragma unroll
    for(int rd=0;rd<4;rd++){
      if(!ovalid(rd)){
        #pragma nounroll
        for(int it=0;it<SPIN_MAX;it++){
          asm volatile("":::"memory");
          #pragma unroll
          for(int i=0;i<4;i++) og[rd][i]=oload(rd,i);
          SB();
          asm volatile("":::"memory");
          if(ovalid(rd)) break;
        }
      }
      const unsigned oa=lbase+ostf(rd,4*j)+lane*16;
      #pragma unroll
      for(int i=0;i<4;i++) ldsw128(oa+i*1024,og[rd][i]);
      SB();
      lbar();
      SB();
    }
  } else {
    const int n=wid;
    __builtin_amdgcn_s_setprio(2);
    const int h=cg*4+n;
    f32x4 ak[8],av[8];
    #pragma unroll
    for(int t=0;t<8;t++){ ak[t]=f32x4{1,1,1,1}; av[t]=f32x4{0,0,0,0}; }
    bf16x8 xf[2][8], wf[2][2];
    const unsigned wp=lbase+(8*g+qq)*128+((((2*n+(pp>>1))^((((qq>>1)&1)<<2)|((g&1)<<1))))<<4)+(pp&1)*8;
    unsigned xa0=xp0+UX(0), xa1=xa0^64, wa=wp+UW(0);
    auto rd1=[&](int ks,int i){
      if(i<2) wf[ks][i]=cat(ldstr(lp(wa+i*8192+ks*4096)),ldstr(lp(wa+i*8192+ks*4096+512)));
      else xf[ks][i-2]=lds128(lp((ks?xa1:xa0)+(i-2)*2048));
    };
    auto mm1=[&](int buf,int i){
      const int t=i>>1;
      if(!(i&1)) ak[t]=MFMA16(xf[buf][t],wf[buf][0],ak[t]);
      else av[t]=MFMA16(xf[buf][t],wf[buf][1],av[t]);
    };
    waitbar<8>();
    #pragma unroll
    for(int i=0;i<10;i++) rd1(0,i);
    SB();
    #pragma unroll 1
    for(int s=0;s<8;s++){
      #pragma unroll
      for(int i=0;i<16;i++){ mm1(0,i); SB(); if(i<10) rd1(1,i); SB(); }
      waitbar<0>();
      if(s==7) break;
      { const int sn=((s+1)&7)*4; xa0=xp0+((TX>>sn)&15u)*16384u; xa1=xa0^64; wa=wp+((TW>>sn)&15u)*16384u; }
      SB();
      #pragma unroll
      for(int i=0;i<16;i++){ mm1(1,i); SB(); if(i<10) rd1(0,i); SB(); }
    }
    #pragma unroll
    for(int i=0;i<16;i++){ mm1(1,i); SB(); }
    SB();
    u32x2 clr0;
    asm volatile("buffer_load_dwordx2 %0, %1, %2, %3 offen sc1":"=v"(clr0):"v"(lane*8),"s"(rs),"s"(WS_CLR+p*512):"memory");
    auto issueO=[&](int i){
      #pragma unroll
      for(int ii=0;ii<4;ii++) dma16(ro,lbase+UO(i)+n*4096+ii*1024,vo,(((i*64)^(c<<7))+ii*16)*1024+c*256+n*64);
    };
    SB();
    {
      f32x4 kv={0,0,0,0}, zz={0,0,0,0};
      const u32x4 ones={0x3f803f80u,0x3f803f80u,0x3f803f80u,0x3f803f80u};
      #pragma unroll
      for(int u=0;u<4;u++){
        u32x4 a={phi2(ak[2*u][0],ak[2*u][1]),phi2(ak[2*u][2],ak[2*u][3]),phi2(ak[2*u+1][0],ak[2*u+1][1]),phi2(ak[2*u+1][2],ak[2*u+1][3])};
        u32x4 v={pk2(av[2*u][0],av[2*u][1]),pk2(av[2*u][2],av[2*u][3]),pk2(av[2*u+1][0],av[2*u+1][1]),pk2(av[2*u+1][2],av[2*u+1][3])};
        kv=MFMA16(__builtin_bit_cast(bf16x8,a),__builtin_bit_cast(bf16x8,v),kv);
        zz=MFMA16(__builtin_bit_cast(bf16x8,ones),__builtin_bit_cast(bf16x8,a),zz);
      }
      const unsigned ph=p*32+h;
      ST8((u32x2{san(pk2(kv[0],kv[1])),san(pk2(kv[2],kv[3]))}),rs,WS_PKV+(ph*16+rt)*512+(g>>1)*256+l15*16+(g&1)*8,0);
      __builtin_amdgcn_raw_buffer_store_b32(san(asu(zz[0])),rs,lane<16?WS_PZ+(ph*16+rt)*64+lane*4:WS_END,0,SC1);
    }
    SB();
    SB();
    rbar();
    asm volatile("s_waitcnt vmcnt(%1)":"+v"(clr0):"n"(2):"memory");
    if(!__all((clr0[0]==tag)&(clr0[1]==tag))){
      #pragma nounroll
      for(int it=0;it<SPIN_MAX;it++){
        clr0=LD8(rs,lane*8,WS_CLR+p*512);
        if(__all((clr0[0]==tag)&(clr0[1]==tag))) break;
        asm volatile("":::"memory");
      }
    }
    u32x4 pk[9];
    const unsigned ph=p*32+h, skv=WS_PKV+ph*8192, szz=WS_PZ+ph*1024, l16=lane*16;
    auto kvfirst=[&](int i){
      if(i<8) asm volatile("buffer_load_dwordx4 %0, %1, %2, %3 offen offset:%4 sc1":"=v"(pk[i]):"v"(l16),"s"(rs),"s"(skv+(i>>2)*4096),"n"((i&3)*1024):"memory");
      else asm volatile("buffer_load_dwordx4 %0, %1, %2, %3 offen sc1":"=v"(pk[8]):"v"(l16),"s"(rs),"s"(szz):"memory");
    };
    const int ao0=n*4096+(4*hh+qq)*64+((((lane>>4)&1)*2+(pp>>1))<<4)+(pp&1)*8, ao1=(ao0+512)^32;
    bf16x8 af[32];
    auto afrd=[&](int pos){ const int o=UO(pos>>2)+(pos&3)*1024; af[pos]=cat(ldstr((ldscp)(smem+o+ao0)),ldstr((ldscp)(smem+o+ao1))); };
    issueO(0);
    SB();
    rbar();
    SB();
    #pragma unroll
    for(int i=0;i<9;i++) kvfirst(i);
    SB();
    issueO(1); issueO(2); issueO(3);
    SB();
    waitvm<21>();
    SB();
    #pragma unroll
    for(int pos=0;pos<4;pos++) afrd(pos);
    u32x4 sel;
    { const unsigned one=((l15>>3)==(g&1))?(0x3f80u<<((l15&1)*16)):0u;
      #pragma unroll
      for(int w=0;w<4;w++) sel[w]=(((l15>>1)&3)==w)?one:0u; }
    SB();
    waitlg();
    asm volatile("s_waitcnt vmcnt(12)":"+v"(pk[0]),"+v"(pk[1]),"+v"(pk[2]),"+v"(pk[3]),"+v"(pk[4]),"+v"(pk[5]),"+v"(pk[6]),"+v"(pk[7]),"+v"(pk[8])::"memory");
    {
      auto kvload=[&](int i){ return i<8 ? LD16(rs,l16+(i&3)*1024,skv+(i>>2)*4096) : LD16(rs,l16,szz); };
      unsigned miss=0;
      #pragma unroll
      for(int i=0;i<9;i++) if(!__all(umax4(pk[i])!=INV)) miss|=1u<<i;
      #pragma nounroll
      for(int it=0;it<SPIN_MAX && miss;it++){
        asm volatile("":::"memory");
        #pragma unroll
        for(int i=0;i<9;i++) if(miss&(1u<<i)) pk[i]=kvload(i);
        #pragma unroll
        for(int i=0;i<9;i++) if(miss&(1u<<i)){ if(__all(umax4(pk[i])!=INV)) miss&=~(1u<<i); }
      }
      f32x4 ka={0,0,0,0}, kb={0,0,0,0};
      #pragma unroll
      for(int i=0;i<4;i++){
        ka=MFMA16(__builtin_bit_cast(bf16x8,sel),__builtin_bit_cast(bf16x8,pk[2*i]),ka);
        kb=MFMA16(__builtin_bit_cast(bf16x8,sel),__builtin_bit_cast(bf16x8,pk[2*i+1]),kb);
      }
      const float z0=sum_hi4(asf(pk[8][0])), z1=sum_hi4(asf(pk[8][1])), z2=sum_hi4(asf(pk[8][2])), z3=sum_hi4(asf(pk[8][3]));
      const f32x4 kv=ka+kb;
      const unsigned fb=lbase+FINB+n*4096;
      ldsw64(fb+(g>>1)*256+l15*16+(g&1)*8,u32x2{pk2(kv[0],kv[1]),pk2(kv[2],kv[3])});
      if(lane<4) ldsw64(fb+512+(lane>>1)*16+(lane&1)*8,u32x2{pk2(z0,z1),pk2(z2,z3)});
    }
    lbar();
    #pragma unroll
    for(int i=4;i<4+WS1;i++) issueO(i);
    waitvm<4*WS1>();
    SB();
    #pragma unroll
    for(int pos=4;pos<16;pos++) afrd(pos);
    SB();
    rbar();
    #pragma unroll
    for(int i=4+WS1;i<4+WS1+WP;i++) issueO(i);
    {
      SB(); waitvm<4*WP>(); SB();
      #pragma unroll
      for(int pos=16;pos<16+4*WS1;pos++) afrd(pos);
      SB(); waitvm<0>(); SB();
      #pragma unroll
      for(int pos=16+4*WS1;pos<32;pos++) afrd(pos);
      SB();
    }
    f32x16 acc[2];
    #pragma unroll
    for(int i=0;i<16;i++){ acc[0][i]=0.f; acc[1][i]=0.f; }
    bf16x8 bfr[8];
    lbar();
    waitvm<0>();
    unsigned ob[2]={lbase+lane*16,lbase+lane*16+65536};
    asm volatile("":"+v"(ob[0]),"+v"(ob[1]));
    auto ordf=[&](int r,int i){ const int o=ostf(r,i); return lds128(lp(ob[o>>16]+(o&65535))); };
    #pragma unroll
    for(int i=0;i<8;i++) bfr[i]=ordf(0,i);
    SB();
    #pragma unroll
    for(int hr=0;hr<8;hr++){
      const int rd=hr>>1;
      if((hr&1) && rd<3){ SB(); rbar(); }
      SB();
      #pragma unroll
      for(int i=0;i<8;i++){
        const int f=(hr&1)*8+i;
        acc[f&1]=MFMA32(af[rd*8+(f>>1)],bfr[i],acc[f&1]);
        if(hr<7){ const int nh=hr+1; bfr[i]=ordf(nh>>1,(nh&1)*8+i); }
        SB();
      }
    }
    if(rt==0 && cg==0 && n==0 && lane==0) __builtin_amdgcn_raw_buffer_store_b32(tag,rs,WS_EPOCH+p*128,0,SC1);
    #pragma unroll
    for(int tb=0;tb<2;tb++){
      const int row=tb*32+(lane&31);
      #pragma unroll
      for(int qd=0;qd<2;qd++){
        unsigned a0=pk2(acc[tb][8*qd+0],acc[tb][8*qd+1]), a1=pk2(acc[tb][8*qd+2],acc[tb][8*qd+3]);
        unsigned b0=pk2(acc[tb][8*qd+4],acc[tb][8*qd+5]), b1=pk2(acc[tb][8*qd+6],acc[tb][8*qd+7]);
        auto s0=__builtin_amdgcn_permlane32_swap(a0,b0,false,false);
        auto s1=__builtin_amdgcn_permlane32_swap(a1,b1,false,false);
        u32x4 v={s0[0],s1[0],s0[1],s1[1]};
        *(u32x4*)(smem+OUTB+row*256+(((n*4+qd*2+hh)^(lane&15))<<4))=v;
      }
    }
    lbar();
    {
      u32x4 v[4];
      #pragma unroll
      for(int i=0;i<4;i++){ const int row=n*16+i*4+g; v[i]=__builtin_bit_cast(u32x4,lds128((ldscp)(smem+OUTB+row*256+((l15^(row&15))<<4)))); }
      #pragma unroll
      for(int i=0;i<4;i++){ const int row=n*16+i*4+g; __builtin_amdgcn_raw_buffer_store_b128(v[i],rout,(unsigned)(tt*64+row)*2048+p*1024+c*256+l15*16,0,OUTAUX); }
    }
  }
}

extern "C" void kernel_launch(void* const* d_in, const int* in_sizes, int n_in, void* d_out, int out_size, void* d_ws, size_t ws_size, hipStream_t stream){
  k_fused<<<256,512,0,stream>>>((const u16*)d_in[0],(const u16*)d_in[1],(const u16*)d_in[2],(const u16*)d_in[3],(const u16*)d_in[4],(const u16*)d_in[5],(char*)d_ws,(u16*)d_out);
}
```
